```python
import torch

def _softmax(x, axis=-1):
    return torch.softmax(x, dim=axis)
def _gelu(x):
    return torch.nn.functional.gelu(x, approximate="tanh")
def _silu(x):
    return torch.nn.functional.silu(x)
def _sigmoid(x):
    return torch.sigmoid(x)
def _layernorm(x, axis=-1, eps=1e-5):
    m = x.mean(dim=axis, keepdim=True)
    v = x.var(dim=axis, keepdim=True, unbiased=False)
    return (x - m) / torch.sqrt(v + eps)
def _scatter_add(tgt, idx, src, axis=0):
    return tgt.clone().index_add_(axis, idx, src)
def _segment_sum(src, seg, n):
    out = torch.zeros((n,) + src.shape[1:], dtype=src.dtype)
    return out.index_add_(0, seg, src)
def _pad(x, pw):
    flat = tuple(v for pair in reversed(pw) for v in pair)
    return torch.nn.functional.pad(x, flat)

kernel_name = 'recurrent_scan__grouped_kv_mqa__glu_gated_reglu__prompt'

def setup_inputs():

    _w = (lambda a: (lambda t: t.to(torch.bfloat16) if t.is_floating_point() else t)(torch.as_tensor(a)))
    def _A_setup():
        B, S, D, H = 1, 2048, 512, 32
        Dh = D // H
        return {
            "x": _w((lambda *s: torch.randn(*s, dtype=torch.bfloat16) * 0.05)(B, S, D)),
            "wq": _w((lambda *s: torch.randn(*s, dtype=torch.bfloat16) * 0.2)(D, H * Dh)),
            "wk": _w((lambda *s: torch.randn(*s, dtype=torch.bfloat16) * 0.2)(D, Dh)),
            "wv": _w((lambda *s: torch.randn(*s, dtype=torch.bfloat16) * 0.2)(D, Dh)),
            "wo": _w((lambda *s: torch.randn(*s, dtype=torch.bfloat16) * 0.2)(H * Dh, D)),
        }
    _A_in = _A_setup()

    def _B_setup():
        B, S, D, F = 1, 2048, 512, 1792
        return {
            "x": _w((lambda *s: torch.randn(*s, dtype=torch.bfloat16) * 0.05)(B, S, D)),
            "W_gate": _w((lambda *s: torch.randn(*s, dtype=torch.bfloat16) * 0.1)(D, F) * (2.0 / D) ** 0.5),
            "W_up": _w((lambda *s: torch.randn(*s, dtype=torch.bfloat16) * 0.1)(D, F) * (2.0 / D) ** 0.5),
            "W_down": _w((lambda *s: torch.randn(*s, dtype=torch.bfloat16) * 0.1)(F, D) * (1.0 / F) ** 0.5),
        }
    _B_in = _B_setup()

    return {
        "A_x": _A_in["x"],
        "A_wq": _A_in["wq"],
        "A_wk": _A_in["wk"],
        "A_wv": _A_in["wv"],
        "A_wo": _A_in["wo"],
        "B_x": _B_in["x"],
        "B_W_gate": _B_in["W_gate"],
        "B_W_up": _B_in["W_up"],
        "B_W_down": _B_in["W_down"],
    }

def reference(A_x, A_wq, A_wk, A_wv, A_wo, B_x, B_W_gate, B_W_up, B_W_down):
    def _A_ref(x, wq, wk, wv, wo):
        B, S, D = x.shape
        H = 32
        Dh = D // H
        q = (lambda x, *d: x.permute(*d))((x @ wq).reshape(B, S, H, Dh), 0, 2, 1, 3)
        k = x @ wk
        v = x @ wv
        scores = torch.einsum("bhqd,bkd->bhqk", q, k) * (Dh ** -0.5)
        p = _softmax(scores, axis=-1)
        o = torch.einsum("bhqk,bkd->bhqd", p, v)
        return (lambda x, *d: x.permute(*d))(o, 0, 2, 1, 3).reshape(B, S, H * Dh) @ wo

    def _B_ref(x, W_gate, W_up, W_down):
        gate = torch.einsum("bsd,df->bsf", x, W_gate)
        up = torch.einsum("bsd,df->bsf", x, W_up)
        gate = gate * (lambda x, dt: x.to(dt))(gate > 0, gate.dtype)
        h = gate * up
        return torch.einsum("bsf,fd->bsd", h, W_down)

    x = A_x
    xn = _layernorm(x, axis=-1)
    s = _A_ref(xn, A_wq, A_wk, A_wv, A_wo)
    g = _silu(_B_ref(xn, B_W_gate, B_W_up, B_W_down))
    return x + s * g
```

```cpp
#include <hip/hip_runtime.h>
#include <hip/hip_bf16.h>
typedef __bf16 bf16;
__global__ void ln_k(const bf16* x, bf16* xn) {
  int row = blockIdx.x; int t = threadIdx.x;
  float v[8]; float s = 0;
  for (int i=0;i<8;i++){ v[i] = (float)x[row*512 + t*8+i]; s += v[i]; }
  for (int o=32;o;o>>=1) s += __shfl_xor(s,o);
  float m = s/512; float q=0;
  for (int i=0;i<8;i++){ q += (v[i]-m)*(v[i]-m); }
  for (int o=32;o;o>>=1) q += __shfl_xor(q,o);
  float r = rsqrtf(q/512 + 1e-5f);
  for (int i=0;i<8;i++) xn[row*512+t*8+i] = (bf16)((v[i]-m)*r);
}
__global__ void gemm_k(const bf16* A, const bf16* B, float* C, int M, int N, int K) {
  int n = blockIdx.x*blockDim.x + threadIdx.x; int m = blockIdx.y;
  if (n>=N) return;
  float acc=0; for (int k=0;k<K;k++) acc += (float)A[m*K+k]*(float)B[k*N+n];
  C[m*N+n]=acc;
}
__global__ void tobf(const float* a, bf16* b, int n){ int i=blockIdx.x*blockDim.x+threadIdx.x; if(i<n) b[i]=(bf16)a[i]; }
__global__ void glu(const float* g, const float* u, bf16* h, int n){ int i=blockIdx.x*blockDim.x+threadIdx.x; if(i<n) h[i]=(bf16)(fmaxf(g[i],0.f)*u[i]); }
__global__ void attn(const bf16* q, const bf16* k, const bf16* v, bf16* o) {
  int r = blockIdx.x*blockDim.x+threadIdx.x;
  float qq[16]; for (int d=0;d<16;d++) qq[d] = (float)q[r*16+d]*0.25f;
  float m=-1e30f;
  for (int j=0;j<2048;j++){ float s=0; for(int d=0;d<16;d++) s+=qq[d]*(float)k[j*16+d]; m=fmaxf(m,s);}
  float l=0, acc[16]; for(int d=0;d<16;d++) acc[d]=0;
  for (int j=0;j<2048;j++){ float s=0; for(int d=0;d<16;d++) s+=qq[d]*(float)k[j*16+d]; float p=(float)(bf16)__expf(s-m); l+=p; for(int d=0;d<16;d++) acc[d]+=p*(float)v[j*16+d]; }
  for(int d=0;d<16;d++) o[r*16+d]=(bf16)(acc[d]/l);
}
__global__ void fin(const bf16* x, const float* s, const float* g, bf16* out, int n){ int i=blockIdx.x*blockDim.x+threadIdx.x; if(i<n){ float gg=g[i]; float si = gg/(1.f+__expf(-gg)); out[i]=(bf16)((float)x[i]+s[i]*si);} }
extern "C" void kernel_launch(void* const* d_in, const int* in_sizes, int n_in, void* d_out, int out_size, void* d_ws, size_t ws_size, hipStream_t stream) {
  const bf16* x=(const bf16*)d_in[0]; const bf16* wq=(const bf16*)d_in[1]; const bf16* wk=(const bf16*)d_in[2]; const bf16* wv=(const bf16*)d_in[3]; const bf16* wo=(const bf16*)d_in[4];
  const bf16* wg=(const bf16*)d_in[6]; const bf16* wu=(const bf16*)d_in[7]; const bf16* wd=(const bf16*)d_in[8];
  char* w=(char*)d_ws; const size_t MB=1<<20;
  bf16* xn=(bf16*)(w); float* f0=(float*)(w+4*MB); float* f1=(float*)(w+24*MB); bf16* q=(bf16*)(w+44*MB); bf16* k=(bf16*)(w+48*MB); bf16* v=(bf16*)(w+49*MB); bf16* o=(bf16*)(w+50*MB); bf16* h=(bf16*)(w+54*MB); float* s=(float*)(w+64*MB); float* g=(float*)(w+72*MB);
  ln_k<<<2048,64,0,stream>>>(x,xn);
  gemm_k<<<dim3(2,2048),256,0,stream>>>(xn,wq,f0,2048,512,512); tobf<<<4096,256,0,stream>>>(f0,q,2048*512);
  gemm_k<<<dim3(1,2048),64,0,stream>>>(xn,wk,f0,2048,16,512); tobf<<<128,256,0,stream>>>(f0,k,2048*16);
  gemm_k<<<dim3(1,2048),64,0,stream>>>(xn,wv,f0,2048,16,512); tobf<<<128,256,0,stream>>>(f0,v,2048*16);
  attn<<<1024,64,0,stream>>>(q,k,v,o);
  gemm_k<<<dim3(2,2048),256,0,stream>>>(o,wo,s,2048,512,512);
  gemm_k<<<dim3(7,2048),256,0,stream>>>(xn,wg,f0,2048,1792,512);
  gemm_k<<<dim3(7,2048),256,0,stream>>>(xn,wu,f1,2048,1792,512);
  glu<<<2048*7,256,0,stream>>>(f0,f1,h,2048*1792);
  gemm_k<<<dim3(2,2048),256,0,stream>>>(h,wd,g,2048,512,1792);
  fin<<<4096,256,0,stream>>>(x,s,g,(bf16*)d_out,2048*512);
}
```

```cpp
#include <hip/hip_runtime.h>
#include <stdint.h>

typedef unsigned short u16;
typedef __attribute__((ext_vector_type(2))) __bf16 bf16x2;
typedef __attribute__((ext_vector_type(8))) __bf16 bf16x8;
typedef __attribute__((ext_vector_type(16))) __bf16 bf16x16;
typedef __attribute__((ext_vector_type(2))) float f32x2;
typedef __attribute__((ext_vector_type(4))) float f32x4;
typedef __attribute__((ext_vector_type(8))) float f32x8;
typedef __attribute__((ext_vector_type(16))) float f32x16;
typedef __attribute__((ext_vector_type(2))) unsigned u32x2;
typedef __attribute__((ext_vector_type(4))) unsigned u32x4;
typedef __attribute__((ext_vector_type(4))) short s16x4;
typedef __attribute__((address_space(3))) s16x4 lds_s16x4;
#define DEV static __device__ __forceinline__
#define BC(T, x) __builtin_bit_cast(T, x)
#define MFMA32(a, b, c) __builtin_amdgcn_mfma_f32_32x32x16_bf16(BC(bf16x8, a), BC(bf16x8, b), c, 0, 0, 0)
#define MFMA16(a, b, c) __builtin_amdgcn_mfma_f32_16x16x32_bf16(BC(bf16x8, a), BC(bf16x8, b), c, 0, 0, 0)
#define SMF(a, b, c) __builtin_amdgcn_smfmac_f32_32x32x32_bf16(BC(bf16x8, a), BC(bf16x16, b), c, 0xDDDD, 0, 0)

constexpr size_t OFF_XN = 0;
constexpr size_t OFF_KF = 2u << 20;
constexpr size_t OFF_VF = OFF_KF + (64u << 10);
constexpr size_t OFF_WQ = OFF_VF + (128u << 10);
constexpr size_t OFF_WO = OFF_WQ + (512u << 10);
constexpr size_t OFF_WD = OFF_WO + (512u << 10);
constexpr size_t OFF_H = OFF_WD + (1792u << 10);
constexpr size_t OFF_O = OFF_H + (7168u << 10);
constexpr int VF_BYTES = 128 * 528;

DEV unsigned pk(float a, float b) { return BC(unsigned, __builtin_convertvector((f32x2){a, b}, bf16x2)); }
DEV float lo16(unsigned u) { return BC(float, u << 16); }
DEV float hi16(unsigned u) { return BC(float, u & 0xffff0000u); }
DEV u32x2 tr_read(const char* p) { return BC(u32x2, __builtin_amdgcn_ds_read_tr16_b64_v4i16((lds_s16x4*)p)); }
DEV u32x4 tr_frag(const char* p0, const char* p1) {
  u32x2 a = tr_read(p0), b = tr_read(p1);
  return (u32x4){a.x, a.y, b.x, b.y};
}
template <int C> DEV float dpp(float v) {
  return BC(float, __builtin_amdgcn_update_dpp(0, BC(int, v), C, 0xf, 0xf, false));
}
DEV float sum16(float v) {
  v += dpp<0x128>(v); v += dpp<0x124>(v); v += dpp<0x122>(v); v += dpp<0x121>(v);
  return v;
}
DEV float other_half(float v) {
  return BC(float, __builtin_amdgcn_ds_bpermute(((threadIdx.x & 63) ^ 32) << 2, BC(int, v)));
}

__global__ __launch_bounds__(256) void k1(const u16* __restrict__ x, const u16* __restrict__ wq,
                                          const u16* __restrict__ wk, const u16* __restrict__ wv,
                                          const u16* __restrict__ wo, const u16* __restrict__ wd, char* __restrict__ ws) {
  __shared__ __attribute__((aligned(16))) char lds[16384 + 32768 + 8192];
  const int b = blockIdx.x, t = threadIdx.x, lane = t & 63, w = __builtin_amdgcn_readfirstlane(t >> 6);
  if (b < 128) {
    const int tok = t >> 4, c0 = t & 15;
    u32x4 xv[4], kw[4], vw[4];
#pragma unroll
    for (int i = 0; i < 4; i++) xv[i] = *(const u32x4*)(x + (b * 16 + tok) * 512 + (c0 + 16 * i) * 8);
#pragma unroll
    for (int i = 0; i < 4; i++) { kw[i] = ((const u32x4*)wk)[t + 256 * i]; vw[i] = ((const u32x4*)wv)[t + 256 * i]; }
    float f[32];
#pragma unroll
    for (int i = 0; i < 4; i++)
#pragma unroll
      for (int j = 0; j < 4; j++) { f[i * 8 + 2 * j] = lo16(xv[i][j]); f[i * 8 + 2 * j + 1] = hi16(xv[i][j]); }
    float s = 0;
#pragma unroll
    for (int i = 0; i < 32; i++) s += f[i];
    const float mean = sum16(s) * (1.0f / 512);
    float q = 0;
#pragma unroll
    for (int i = 0; i < 32; i++) { f[i] -= mean; q += f[i] * f[i]; }
    const float rstd = 1.0f / sqrtf(sum16(q) * (1.0f / 512) + 1e-5f);
#pragma unroll
    for (int i = 0; i < 4; i++) {
      u32x4 o;
#pragma unroll
      for (int j = 0; j < 4; j++) o[j] = pk(f[i * 8 + 2 * j] * rstd, f[i * 8 + 2 * j + 1] * rstd);
      *(u32x4*)(lds + (c0 + 16 * i) * 256 + ((tok ^ c0) * 16)) = o;
    }
#pragma unroll
    for (int i = 0; i < 4; i++) {
      *(u32x4*)(lds + 16384 + (t + 256 * i) * 16) = kw[i];
      *(u32x4*)(lds + 32768 + (t + 256 * i) * 16) = vw[i];
    }
    __syncthreads();
    const int n = lane & 15, g = lane >> 4;
    f32x4 ak = {0, 0, 0, 0}, av = {0, 0, 0, 0};
    char* xnf = ws + OFF_XN + (b >> 1) * 32768 + ((b & 1) * 16 + n) * 16;
#pragma unroll
    for (int i = 0; i < 4; i++) {
      const int S = 4 * w + i, c = 4 * S + g;
      const u32x4 xf = *(const u32x4*)(lds + c * 256 + ((n ^ (c & 15)) * 16));
      const char* pw = lds + 16384 + (32 * S + 8 * g + (n >> 2)) * 32 + (n & 3) * 8;
      const u32x4 kf = tr_frag(pw, pw + 128), vf = tr_frag(pw + 16384, pw + 16384 + 128);
      ak = MFMA16(kf, xf, ak);
      av = MFMA16(xf, vf, av);
      *(u32x4*)(xnf + c * 512) = xf;
    }
    f32x4* red = (f32x4*)(lds + 49152);
    red[(w * 2 + 0) * 64 + lane] = ak;
    red[(w * 2 + 1) * 64 + lane] = av;
    __syncthreads();
    if (w < 2) {
      const f32x4 r = red[(0 + w) * 64 + lane] + red[(2 + w) * 64 + lane] + red[(4 + w) * 64 + lane] + red[(6 + w) * 64 + lane];
      const u32x2 o = {pk(r[0], r[1]), pk(r[2], r[3])};
      if (w == 0) *(u32x2*)(ws + OFF_KF + ((b >> 1) * 64 + (g & 1) * 32 + (b & 1) * 16 + n) * 16 + (g >> 1) * 8) = o;
      else *(u32x2*)(ws + OFF_VF + b * 528 + (g >> 1) * 256 + n * 16 + (g & 1) * 8) = o;
    } else if (t == 128) {
      *(u32x4*)(ws + OFF_VF + b * 528 + 512) = (u32x4){0x3f803f80u, 0x3f803f80u, 0x3f803f80u, 0x3f803f80u};
    }
  } else {
    const int sl = b - 128;
    const u16* src; char* dst; int s, ns;
    if (sl < 32) { src = wq; s = sl; ns = 32; dst = ws + OFF_WQ; }
    else if (sl < 64) { src = wo; s = sl - 32; ns = 32; dst = ws + OFF_WO; }
    else { src = wd; s = sl - 64; ns = 112; dst = ws + OFF_WD; }
    u32x4 v[4];
#pragma unroll
    for (int i = 0; i < 4; i++) v[i] = ((const u32x4*)(src + s * 16 * 512))[t + 256 * i];
#pragma unroll
    for (int i = 0; i < 4; i++) *(u32x4*)(lds + (w + 4 * i) * 1088 + lane * 16) = v[i];
    __syncthreads();
    const int h = lane >> 5, blk = (lane >> 4) & 1, q = (lane & 15) >> 2, p = lane & 3;
    const int r0 = sl < 32 ? 8 * h + q : 4 * h + q, dr = sl < 32 ? 4 : 8;
#pragma unroll
    for (int i = 0; i < 4; i++) {
      const int j = 4 * w + i;
      const char* pp = lds + r0 * 1088 + j * 64 + blk * 32 + p * 8;
      *(u32x4*)(dst + (j * ns + s) * 1024 + lane * 16) = tr_frag(pp, pp + dr * 1088);
    }
  }
}

template <int NS, int DEPTH>
DEV void tile64(const char* A0, const char* A1, const char* B0, const char* B1, int lane, f32x16 (&acc)[2][2]) {
  u32x4 r[DEPTH][4];
  const int lo = lane * 16;
#pragma unroll
  for (int s = 0; s < DEPTH; s++) {
    r[s][0] = *(const u32x4*)(A0 + s * 1024 + lo); r[s][2] = *(const u32x4*)(B0 + s * 1024 + lo);
    r[s][1] = *(const u32x4*)(A1 + s * 1024 + lo); r[s][3] = *(const u32x4*)(B1 + s * 1024 + lo);
  }
  __builtin_amdgcn_sched_barrier(0);
#pragma unroll
  for (int s = 0; s < NS; s++) {
    const int d = s % DEPTH;
    acc[0][0] = MFMA32(r[d][0], r[d][2], acc[0][0]);
    acc[1][0] = MFMA32(r[d][1], r[d][2], acc[1][0]);
    acc[0][1] = MFMA32(r[d][0], r[d][3], acc[0][1]);
    acc[1][1] = MFMA32(r[d][1], r[d][3], acc[1][1]);
    if (s + DEPTH < NS) {
      const int o = (s + DEPTH) * 1024 + lo;
      r[d][0] = *(const u32x4*)(A0 + o); r[d][2] = *(const u32x4*)(B0 + o);
      r[d][1] = *(const u32x4*)(A1 + o); r[d][3] = *(const u32x4*)(B1 + o);
    }
    __builtin_amdgcn_sched_barrier(0);
  }
}
DEV void zero4(f32x16 (&acc)[2][2]) {
#pragma unroll
  for (int i = 0; i < 2; i++)
#pragma unroll
    for (int j = 0; j < 2; j++)
#pragma unroll
      for (int r = 0; r < 16; r++) acc[i][j][r] = 0.f;
}
constexpr int RP = 1056;
DEV void red_write(char* lds, int w, int lane, const f32x16 (&acc)[2][2]) {
#pragma unroll
  for (int j = 0; j < 2; j++)
#pragma unroll
    for (int i = 0; i < 2; i++)
#pragma unroll
      for (int q = 0; q < 4; q++)
        *(f32x4*)(lds + (((w * 2 + j) * 2 + i) * 4 + q) * RP + lane * 16) =
            (f32x4){acc[i][j][4 * q], acc[i][j][4 * q + 1], acc[i][j][4 * q + 2], acc[i][j][4 * q + 3]};
}
DEV void red_read8(const char* lds, int j, int cg, int tk, float (&o)[8]) {
  f32x4 a = {0, 0, 0, 0}, b = {0, 0, 0, 0};
#pragma unroll
  for (int w = 0; w < 4; w++) {
    const char* p = lds + ((w * 2 + j) * 8 + cg) * RP + tk * 16;
    a += *(const f32x4*)p; b += *(const f32x4*)(p + 512);
  }
#pragma unroll
  for (int e = 0; e < 4; e++) { o[e] = a[e]; o[4 + e] = b[e]; }
}

constexpr int LDS2 = 64 * RP;

DEV void exp8(f32x8& p, const f32x16& s, int o) {
#pragma unroll
  for (int i = 0; i < 8; i++) p[i] = __builtin_amdgcn_exp2f(s[o + i]);
}
DEV float max16(const f32x16& s) {
  float m = fmaxf(s[0], s[1]);
#pragma unroll
  for (int i = 2; i < 16; i += 2) m = fmaxf(m, fmaxf(s[i], s[i + 1]));
  return m;
}
DEV f32x16 splat16(float v) { f32x16 c;
#pragma unroll
  for (int i = 0; i < 16; i++) c[i] = v;
  return c;
}

DEV void attention(char* lds, char* __restrict__ ws, int tbp, int hg) {
  const int t = threadIdx.x, lane = t & 63, w = __builtin_amdgcn_readfirstlane(t >> 6), tb = w >> 1, kh = w & 1;
  u32x4 qf[4];
  {
    f32x16 acc[2][2]; zero4(acc);
    const char* A = ws + OFF_WQ + (hg * 2 * 32 + 8 * w) * 1024;
    const char* B = ws + OFF_XN + (tbp * 2 * 32 + 8 * w) * 1024;
    tile64<8, 8>(A, A + 32768, B, B + 32768, lane, acc);
    red_write(lds, w, lane, acc);
    __syncthreads();
    const float sc = 0.25f * 1.4426950408889634f;
#pragma unroll
    for (int hd = 0; hd < 4; hd++) {
      f32x4 a = {0, 0, 0, 0}, b = {0, 0, 0, 0};
#pragma unroll
      for (int ww = 0; ww < 4; ww++) {
        const char* p = lds + ((ww * 2 + tb) * 8 + hd * 2) * RP + lane * 16;
        a += *(const f32x4*)p; b += *(const f32x4*)(p + RP);
      }
      qf[hd] = (u32x4){pk(a[0] * sc, a[1] * sc), pk(a[2] * sc, a[3] * sc), pk(b[0] * sc, b[1] * sc), pk(b[2] * sc, b[3] * sc)};
    }
  }
  const char* KF = ws + OFF_KF + lane * 16;
  const __amdgpu_buffer_rsrc_t vrs = __builtin_amdgcn_make_buffer_rsrc(ws + OFF_VF, 0, VF_BYTES, 0x00020000);
  const int m_ = lane & 31;
  const int vo = m_ < 16 ? (lane >> 5) * 256 + m_ * 16 : ((m_ == 16 || m_ == 20) ? 512 : 0x40000000);
#define LDV(g) BC(u32x4, __builtin_amdgcn_raw_buffer_load_b128(vrs, vo, (g) * 528, 0))
#define LDK(kb) (*(const u32x4*)(KF + (kb) * 1024))
  f32x16 o0, o1, o2, o3;
  const u32x4 q0 = qf[0], q1 = qf[1], q2 = qf[2], q3 = qf[3];
  float mref;
  {
    const u32x4 k0 = LDK(0), k1 = LDK(1);
    const f32x16 z = splat16(0.f);
    float m = -3.0e38f;
#pragma unroll
    for (int hd = 0; hd < 4; hd++) m = fmaxf(m, fmaxf(max16(MFMA32(k0, qf[hd], z)), max16(MFMA32(k1, qf[hd], z))));
    mref = fmaxf(m, other_half(m)) + 20.f;
  }
  {
    const f32x16 c = splat16(-mref);
    o0 = o1 = o2 = o3 = splat16(0.f);
    const int kb0 = kh * 32;
    u32x4 kf[2], va[2], vb[2];
    kf[0] = LDK(kb0); va[0] = LDV(2 * kb0); vb[0] = LDV(2 * kb0 + 1);
    kf[1] = LDK(kb0 + 1); va[1] = LDV(2 * kb0 + 2); vb[1] = LDV(2 * kb0 + 3);
    f32x16 sa = MFMA32(kf[0], q0, c), sb;
    for (int it = 0; it < 32; it += 2) {
#pragma unroll
      for (int u = 0; u < 2; u++) {
        const int nk = (kb0 + it + u + 2) & 63;
        const u32x4 k = kf[u], v0 = va[u], v1 = vb[u];
        kf[u] = LDK(nk); va[u] = LDV(2 * nk); vb[u] = LDV(2 * nk + 1);
        f32x8 p0, p1;
        sb = MFMA32(k, q1, c);
        exp8(p0, sa, 0); o0 = SMF(v0, p0, o0);
        exp8(p1, sa, 8); o0 = SMF(v1, p1, o0);
        sa = MFMA32(k, q2, c);
        exp8(p0, sb, 0); o1 = SMF(v0, p0, o1);
        exp8(p1, sb, 8); o1 = SMF(v1, p1, o1);
        sb = MFMA32(k, q3, c);
        exp8(p0, sa, 0); o2 = SMF(v0, p0, o2);
        exp8(p1, sa, 8); o2 = SMF(v1, p1, o2);
        sa = MFMA32(kf[u ^ 1], q0, c);
        exp8(p0, sb, 0); o3 = SMF(v0, p0, o3);
        exp8(p1, sb, 8); o3 = SMF(v1, p1, o3);
      }
    }
  }
  char* xch = lds;
  float r[2][9];
  bool second = false;
  for (;;) {
    __syncthreads();
#pragma unroll
    for (int e = 0; e < 2; e++) {
      const f32x16 s = e ? (kh ? o1 : o3) : (kh ? o0 : o2);
      char* p = xch + ((w * 2 + e) * 3) * 1024 + lane * 16;
      *(f32x4*)p = (f32x4){s[0], s[1], s[2], s[3]};
      *(f32x4*)(p + 1024) = (f32x4){s[4], s[5], s[6], s[7]};
      *(float*)(xch + ((w * 2 + e) * 3 + 2) * 1024 + lane * 4) = s[8];
    }
    __syncthreads();
    bool bad = false;
#pragma unroll
    for (int e = 0; e < 2; e++) {
      const f32x16 s = e ? (kh ? o3 : o1) : (kh ? o2 : o0);
      const char* p = xch + (((w ^ 1) * 2 + e) * 3) * 1024 + lane * 16;
      const f32x4 a = *(const f32x4*)p, b = *(const f32x4*)(p + 1024);
      const float l = s[8] + *(const float*)(xch + (((w ^ 1) * 2 + e) * 3 + 2) * 1024 + lane * 4);
      float am = 0.f;
#pragma unroll
      for (int i = 0; i < 4; i++) {
        r[e][i] = s[i] + a[i]; r[e][4 + i] = s[4 + i] + b[i];
        am += fabsf(r[e][i]) + fabsf(r[e][4 + i]);
      }
      r[e][8] = l;
      bad |= !(l >= 0x1p-100f && l <= 0x1p120f && am <= 0x1p126f);
    }
    if (second) break;
    int* vote = (int*)(lds + 30720);
    if (lane == 0) vote[w] = 0;
    if (bad) vote[w] = 1;
    __syncthreads();
    if (__builtin_amdgcn_readfirstlane(vote[0] | vote[1] | vote[2] | vote[3]) == 0) break;
    second = true;
    float* mx = (float*)(lds + 32768);
    for (int hd = 0; hd < 4; hd++) {
      const u32x4 q = hd == 0 ? q0 : hd == 1 ? q1 : hd == 2 ? q2 : q3;
      const f32x16 z = splat16(0.f);
      float m = -3.0e38f;
      for (int kb = kh * 32; kb < kh * 32 + 32; kb++) m = fmaxf(m, max16(MFMA32(LDK(kb), q, z)));
      m = fmaxf(m, other_half(m));
      mx[(hd * 4 + w) * 64 + lane] = m;
      __syncthreads();
      m = fmaxf(m, mx[(hd * 4 + (w ^ 1)) * 64 + lane]);
      const f32x16 c = splat16(-m);
      f32x16 oo = splat16(0.f);
      for (int kb = kh * 32; kb < kh * 32 + 32; kb++) {
        const f32x16 s = MFMA32(LDK(kb), q, c);
        f32x8 p0, p1;
        exp8(p0, s, 0); oo = SMF(LDV(2 * kb), p0, oo);
        exp8(p1, s, 8); oo = SMF(LDV(2 * kb + 1), p1, oo);
      }
      if (hd == 0) o0 = oo; else if (hd == 1) o1 = oo; else if (hd == 2) o2 = oo; else o3 = oo;
    }
  }
#pragma unroll
  for (int e = 0; e < 2; e++) {
    const float il = 1.0f / r[e][8];
    const u32x4 v = {pk(r[e][0] * il, r[e][1] * il), pk(r[e][2] * il, r[e][3] * il), pk(r[e][4] * il, r[e][5] * il),
                     pk(r[e][6] * il, r[e][7] * il)};
    *(u32x4*)(ws + OFF_O + ((tbp * 2 + tb) * 32 + hg * 4 + 2 * kh + e) * 1024 + lane * 16) = v;
  }
#undef LDV
#undef LDK
}

constexpr int PLANE = 64 * 224, UPOFF = PLANE + 128, STAGE = 2 * PLANE + 128;
DEV void gemm1(char* lds, const u16* __restrict__ wg, const u16* __restrict__ wu, char* __restrict__ ws, int mt, int nt) {
  const int t = threadIdx.x, lane = t & 63, w = __builtin_amdgcn_readfirstlane(t >> 6), half = w >> 1, side = w & 1;
  const char* wsrc = (const char*)(half ? wu : wg) + nt * 224;
  int go[7];
#pragma unroll
  for (int i = 0; i < 7; i++) { const int id = (side * 7 + i) * 64 + lane, row = id / 14; go[i] = row * 3584 + (id - row * 14) * 16; }
  char* ldst = lds + half * UPOFF + side * 7168;
  const char* B0 = ws + OFF_XN + (mt * 4 + half * 2 + side) * 32768 + lane * 16;
  const char* B1 = ws + OFF_XN + (mt * 4 + half * 2 + (side ^ 1)) * 32768 + lane * 16;
  const int h = lane >> 5, blk = (lane >> 4) & 1, q = (lane & 15) >> 2, p = lane & 3;
  const int ao = blk * UPOFF + (8 * h + q) * 224 + p * 8;
  const int a3 = ao + 3 * 32, aj = ao + side * 4 * 32;
  f32x16 acc[3][2], acs;
#pragma unroll
  for (int r = 0; r < 16; r++) { acs[r] = 0.f;
#pragma unroll
    for (int i = 0; i < 3; i++) acc[i][0][r] = acc[i][1][r] = 0.f; }
  u32x4 bb[2][4][2];
  auto issue = [&](int c, int st) {
#pragma unroll
    for (int i = 0; i < 7; i++)
      __builtin_amdgcn_global_load_lds((const __attribute__((address_space(1))) unsigned*)(wsrc + (size_t)c * (64 * 3584) + go[i]),
                                       (__attribute__((address_space(3))) unsigned*)(ldst + st * STAGE + i * 1024), 16, 0, 0);
#pragma unroll
    for (int s = 0; s < 4; s++) {
      bb[st][s][0] = *(const u32x4*)(B0 + (c * 4 + s) * 1024);
      bb[st][s][1] = *(const u32x4*)(B1 + (c * 4 + s) * 1024);
    }
  };
  auto compute = [&](int st) {
    const char* sb = lds + st * STAGE;
#pragma unroll
    for (int s = 0; s < 4; s++) {
      const char* pa = sb + s * (16 * 224);
      const u32x4 f3 = tr_frag(pa + a3, pa + a3 + 896);
      u32x4 f[3];
#pragma unroll
      for (int i = 0; i < 3; i++) f[i] = tr_frag(pa + aj + i * 32, pa + aj + i * 32 + 896);
      acs = MFMA32(f3, bb[st][s][0], acs);
#pragma unroll
      for (int i = 0; i < 3; i++) {
        acc[i][0] = MFMA32(f[i], bb[st][s][0], acc[i][0]);
        acc[i][1] = MFMA32(f[i], bb[st][s][1], acc[i][1]);
      }
    }
  };
  issue(0, 0);
  __builtin_amdgcn_s_waitcnt(0x0f70);
  __syncthreads();
  for (int c = 0; c < 8; c += 2) {
    issue(c + 1, 1);
    __builtin_amdgcn_sched_barrier(0);
    compute(0);
    __builtin_amdgcn_s_waitcnt(0x0f70);
    __syncthreads();
    if (c + 2 < 8) issue(c + 2, 0);
    __builtin_amdgcn_sched_barrier(0);
    compute(1);
    __builtin_amdgcn_s_waitcnt(0x0f70);
    __syncthreads();
  }
  auto put = [&](const f32x16& a, int tb, int fb) {
    float hv[8];
#pragma unroll
    for (int r = 0; r < 8; r++) hv[r] = fmaxf(a[r], 0.f) * a[8 + r];
    *(u32x4*)(ws + OFF_H + (size_t)(tb * 112 + fb) * 1024 + lane * 16) =
        (u32x4){pk(hv[0], hv[1]), pk(hv[2], hv[3]), pk(hv[4], hv[5]), pk(hv[6], hv[7])};
  };
  const int tb0 = mt * 4 + half * 2 + side, tb1 = tb0 ^ 1, fb = nt * 7;
  put(acs, tb0, fb + 3);
#pragma unroll
  for (int i = 0; i < 3; i++) { put(acc[i][0], tb0, fb + side * 4 + i); put(acc[i][1], tb1, fb + side * 4 + i); }
}

__global__ __launch_bounds__(256, 2) void k2(const u16* __restrict__ wg, const u16* __restrict__ wu, char* __restrict__ ws) {
  __shared__ __attribute__((aligned(16))) char lds[LDS2];
  const int b = blockIdx.x, xc = b & 7, r = (b >> 3) & 31;
  if (b < 256) attention(lds, ws, (xc & 1) * 16 + (r & 15), (xc >> 1) * 2 + (r >> 4));
  else gemm1(lds, wg, wu, ws, (xc & 1) * 8 + (r & 7), (xc >> 1) * 4 + (r >> 3));
}

__global__ __launch_bounds__(256) void k3(const u16* __restrict__ x, const char* __restrict__ ws, u16* __restrict__ out) {
  __shared__ __attribute__((aligned(16))) char lds[2 * LDS2];
  const int b = blockIdx.x, t = threadIdx.x, lane = t & 63, w = __builtin_amdgcn_readfirstlane(t >> 6);
  const int tbp = (b & 7) * 4 + ((b >> 3) & 3), ct = b >> 5;
  f32x16 acc[2][2];
  {
    zero4(acc);
    const char* A = ws + OFF_WO + (ct * 2 * 32 + 8 * w) * 1024;
    const char* B = ws + OFF_O + (tbp * 2 * 32 + 8 * w) * 1024;
    tile64<8, 8>(A, A + 32768, B, B + 32768, lane, acc);
    red_write(lds + LDS2, w, lane, acc);
  }
  {
    zero4(acc);
    const char* A = ws + OFF_WD + (ct * 2 * 112 + 28 * w) * 1024;
    const char* B = ws + OFF_H + (size_t)(tbp * 2 * 112 + 28 * w) * 1024;
    tile64<28, 8>(A, A + 112 * 1024, B, B + 112 * 1024, lane, acc);
    red_write(lds, w, lane, acc);
  }
  const int cg = t & 7, tk = t >> 3;
  u32x4 xv[2];
#pragma unroll
  for (int j = 0; j < 2; j++) xv[j] = *(const u32x4*)(x + (tbp * 64 + j * 32 + tk) * 512 + ct * 64 + cg * 8);
  __syncthreads();
#pragma unroll
  for (int j = 0; j < 2; j++) {
    float d[8], s[8], o[8];
    red_read8(lds, j, cg, tk, d);
    red_read8(lds + LDS2, j, cg, tk, s);
#pragma unroll
    for (int e = 0; e < 8; e++) {
      const float g = d[e] / (1.0f + __expf(-d[e]));
      const float xx = (e & 1) ? hi16(xv[j][e >> 1]) : lo16(xv[j][e >> 1]);
      o[e] = xx + s[e] * g;
    }
    *(u32x4*)(out + (tbp * 64 + j * 32 + tk) * 512 + ct * 64 + cg * 8) =
        (u32x4){pk(o[0], o[1]), pk(o[2], o[3]), pk(o[4], o[5]), pk(o[6], o[7])};
  }
}

extern "C" void kernel_launch(void* const* d_in, const int* in_sizes, int n_in, void* d_out, int out_size, void* d_ws,
                              size_t ws_size, hipStream_t stream) {
  const u16* x = (const u16*)d_in[0];
  char* ws = (char*)d_ws;
  k1<<<304, 256, 0, stream>>>(x, (const u16*)d_in[1], (const u16*)d_in[2], (const u16*)d_in[3], (const u16*)d_in[4],
                              (const u16*)d_in[8], ws);
  k2<<<512, 256, 0, stream>>>((const u16*)d_in[6], (const u16*)d_in[7], ws);
  k3<<<256, 256, 0, stream>>>(x, ws, (u16*)d_out);
}
```

```cpp
#include <hip/hip_runtime.h>
#include <stdint.h>

typedef unsigned short u16;
typedef __attribute__((ext_vector_type(2))) __bf16 bf16x2;
typedef __attribute__((ext_vector_type(8))) __bf16 bf16x8;
typedef __attribute__((ext_vector_type(16))) __bf16 bf16x16;
typedef __attribute__((ext_vector_type(2))) float f32x2;
typedef __attribute__((ext_vector_type(4))) float f32x4;
typedef __attribute__((ext_vector_type(8))) float f32x8;
typedef __attribute__((ext_vector_type(16))) float f32x16;
typedef __attribute__((ext_vector_type(2))) unsigned u32x2;
typedef __attribute__((ext_vector_type(4))) unsigned u32x4;
typedef __attribute__((ext_vector_type(4))) short s16x4;
typedef __attribute__((address_space(3))) s16x4 lds_s16x4;
typedef __attribute__((address_space(1))) unsigned gu32;
typedef __attribute__((address_space(3))) unsigned lu32;
typedef __amdgpu_buffer_rsrc_t rsrc_t;
#define ATT_FIRST 0
#define WARM 1
#define SPACING 1
#define DEV static __device__ __forceinline__
#define BC(T, x) __builtin_bit_cast(T, x)
#define MFMA32(a, b, c) __builtin_amdgcn_mfma_f32_32x32x16_bf16(BC(bf16x8, a), BC(bf16x8, b), c, 0, 0, 0)
#define MFMA16(a, b, c) __builtin_amdgcn_mfma_f32_16x16x32_bf16(BC(bf16x8, a), BC(bf16x8, b), c, 0, 0, 0)
#define SMF(a, b, c) __builtin_amdgcn_smfmac_f32_32x32x32_bf16(BC(bf16x8, a), BC(bf16x16, b), c, 0xDDDD, 0, 0)

constexpr size_t OFF_XN = 0;
constexpr size_t OFF_KF = 2u << 20;
constexpr size_t OFF_VF = OFF_KF + (64u << 10);
constexpr size_t OFF_WQ = OFF_VF + (128u << 10);
constexpr size_t OFF_WO = OFF_WQ + (512u << 10);
constexpr size_t OFF_WD = OFF_WO + (512u << 10);
constexpr size_t OFF_H = OFF_WD + (1792u << 10);
constexpr size_t OFF_O = OFF_H + (7168u << 10);
constexpr size_t OFF_CNT = OFF_O + (2048u << 10);
constexpr int VF_BYTES = 128 * 528;

#define TS(k, i)
DEV unsigned pk(float a, float b) { return BC(unsigned, __builtin_convertvector((f32x2){a, b}, bf16x2)); }
DEV float lo16(unsigned u) { return BC(float, u << 16); }
DEV float hi16(unsigned u) { return BC(float, u & 0xffff0000u); }
DEV u32x2 tr_read(const char* p) { return BC(u32x2, __builtin_amdgcn_ds_read_tr16_b64_v4i16((lds_s16x4*)p)); }
DEV u32x4 tr_frag(const char* p0, const char* p1) {
  u32x2 a = tr_read(p0), b = tr_read(p1);
  return (u32x4){a.x, a.y, b.x, b.y};
}
template <int C> DEV float dpp(float v) {
  return BC(float, __builtin_amdgcn_update_dpp(0, BC(int, v), C, 0xf, 0xf, false));
}
DEV float sum16(float v) {
  v += dpp<0x128>(v); v += dpp<0x124>(v); v += dpp<0x122>(v); v += dpp<0x121>(v);
  return v;
}
DEV float other_half(float v) {
  return BC(float, __builtin_amdgcn_ds_bpermute(((threadIdx.x & 63) ^ 32) << 2, BC(int, v)));
}

__global__ __launch_bounds__(256) void k1(const u16* __restrict__ x, const u16* __restrict__ wq,
                                          const u16* __restrict__ wk, const u16* __restrict__ wv,
                                          const u16* __restrict__ wo, const u16* __restrict__ wd, char* __restrict__ ws) {
  __shared__ __attribute__((aligned(16))) char lds[16384 + 32768 + 8192];
  const int b = blockIdx.x, t = threadIdx.x, lane = t & 63, w = __builtin_amdgcn_readfirstlane(t >> 6);
  TS(0, 0);
  if (b < 128) {
    const int tok = t >> 4, c0 = t & 15;
    u32x4 xv[4], kw[4], vw[4];
#pragma unroll
    for (int i = 0; i < 4; i++) xv[i] = *(const u32x4*)(x + (b * 16 + tok) * 512 + (c0 + 16 * i) * 8);
#pragma unroll
    for (int i = 0; i < 4; i++) { kw[i] = ((const u32x4*)wk)[t + 256 * i]; vw[i] = ((const u32x4*)wv)[t + 256 * i]; }
    float f[32];
#pragma unroll
    for (int i = 0; i < 4; i++)
#pragma unroll
      for (int j = 0; j < 4; j++) { f[i * 8 + 2 * j] = lo16(xv[i][j]); f[i * 8 + 2 * j + 1] = hi16(xv[i][j]); }
    float s = 0;
#pragma unroll
    for (int i = 0; i < 32; i++) s += f[i];
    const float mean = sum16(s) * (1.0f / 512);
    float q = 0;
#pragma unroll
    for (int i = 0; i < 32; i++) { f[i] -= mean; q += f[i] * f[i]; }
    const float rstd = 1.0f / sqrtf(sum16(q) * (1.0f / 512) + 1e-5f);
#pragma unroll
    for (int i = 0; i < 4; i++) {
      u32x4 o;
#pragma unroll
      for (int j = 0; j < 4; j++) o[j] = pk(f[i * 8 + 2 * j] * rstd, f[i * 8 + 2 * j + 1] * rstd);
      *(u32x4*)(lds + (c0 + 16 * i) * 256 + ((tok ^ c0) * 16)) = o;
    }
#pragma unroll
    for (int i = 0; i < 4; i++) {
      *(u32x4*)(lds + 16384 + (t + 256 * i) * 16) = kw[i];
      *(u32x4*)(lds + 32768 + (t + 256 * i) * 16) = vw[i];
    }
    TS(0, 1);
    __syncthreads();
    const int n = lane & 15, g = lane >> 4;
    f32x4 ak = {0, 0, 0, 0}, av = {0, 0, 0, 0};
    char* xnf = ws + OFF_XN + (b >> 1) * 32768 + ((b & 1) * 16 + n) * 16;
#pragma unroll
    for (int i = 0; i < 4; i++) {
      const int S = 4 * w + i, c = 4 * S + g;
      const u32x4 xf = *(const u32x4*)(lds + c * 256 + ((n ^ (c & 15)) * 16));
      const char* pw = lds + 16384 + (32 * S + 8 * g + (n >> 2)) * 32 + (n & 3) * 8;
      const u32x4 kf = tr_frag(pw, pw + 128), vf = tr_frag(pw + 16384, pw + 16384 + 128);
      ak = MFMA16(kf, xf, ak);
      av = MFMA16(xf, vf, av);
      *(u32x4*)(xnf + c * 512) = xf;
    }
    f32x4* red = (f32x4*)(lds + 49152);
    red[(w * 2 + 0) * 64 + lane] = ak;
    red[(w * 2 + 1) * 64 + lane] = av;
    __syncthreads();
    if (w < 2) {
      const f32x4 r = red[(0 + w) * 64 + lane] + red[(2 + w) * 64 + lane] + red[(4 + w) * 64 + lane] + red[(6 + w) * 64 + lane];
      const u32x2 o = {pk(r[0], r[1]), pk(r[2], r[3])};
      if (w == 0) *(u32x2*)(ws + OFF_KF + ((b >> 1) * 64 + (g & 1) * 32 + (b & 1) * 16 + n) * 16 + (g >> 1) * 8) = o;
      else *(u32x2*)(ws + OFF_VF + b * 528 + (g >> 1) * 256 + n * 16 + (g & 1) * 8) = o;
    } else if (t == 192) {
      if (b < 48) *(unsigned*)(ws + OFF_CNT + b * 128) = 0;
    } else if (t == 128) {
      *(u32x4*)(ws + OFF_VF + b * 528 + 512) = (u32x4){0x3f803f80u, 0x3f803f80u, 0x3f803f80u, 0x3f803f80u};
    }
  } else {
    const int sl = b - 128;
    const u16* src; char* dst; int s, ns;
    if (sl < 32) { src = wq; s = sl; ns = 32; dst = ws + OFF_WQ; }
    else if (sl < 64) { src = wo; s = sl - 32; ns = 32; dst = ws + OFF_WO; }
    else { src = wd; s = sl - 64; ns = 112; dst = ws + OFF_WD; }
    u32x4 v[4];
#pragma unroll
    for (int i = 0; i < 4; i++) v[i] = ((const u32x4*)(src + s * 16 * 512))[t + 256 * i];
#pragma unroll
    for (int i = 0; i < 4; i++) *(u32x4*)(lds + (w + 4 * i) * 1088 + lane * 16) = v[i];
    __syncthreads();
    const int h = lane >> 5, blk = (lane >> 4) & 1, q = (lane & 15) >> 2, p = lane & 3;
    const int r0 = sl < 32 ? 8 * h + q : 4 * h + q, dr = sl < 32 ? 4 : 8;
#pragma unroll
    for (int i = 0; i < 4; i++) {
      const int j = 4 * w + i;
      const char* pp = lds + r0 * 1088 + j * 64 + blk * 32 + p * 8;
      *(u32x4*)(dst + (j * ns + s) * 1024 + lane * 16) = tr_frag(pp, pp + dr * 1088);
    }
  }
  TS(0, 2);
}

DEV rsrc_t mkrsrc(const char* p, int bytes) { return __builtin_amdgcn_make_buffer_rsrc((void*)p, 0, bytes, 0x00020000); }
template <int AUX> DEV u32x4 bld(rsrc_t r, int vo, int so) { return BC(u32x4, __builtin_amdgcn_raw_buffer_load_b128(r, vo, so, AUX)); }
template <int DEPTH> DEV void tileA(u32x4 (&r)[DEPTH][4], rsrc_t ra, int a0, int a1, int lo) {
#pragma unroll
  for (int s = 0; s < DEPTH; s++) { r[s][0] = bld<0>(ra, lo, a0 + s * 1024); r[s][1] = bld<0>(ra, lo, a1 + s * 1024); }
}
template <int NS, int DEPTH, int AUX>
DEV void tile64(u32x4 (&r)[DEPTH][4], rsrc_t ra, int a0, int a1, rsrc_t rb, int b0, int b1, int lo, f32x16 (&acc)[2][2]) {
#pragma unroll
  for (int s = 0; s < DEPTH; s++) { r[s][2] = bld<AUX>(rb, lo, b0 + s * 1024); r[s][3] = bld<AUX>(rb, lo, b1 + s * 1024); }
  __builtin_amdgcn_sched_barrier(0);
#pragma unroll
  for (int s = 0; s < NS; s++) {
    const int d = s % DEPTH;
    acc[0][0] = MFMA32(r[d][0], r[d][2], acc[0][0]);
    acc[1][0] = MFMA32(r[d][1], r[d][2], acc[1][0]);
    acc[0][1] = MFMA32(r[d][0], r[d][3], acc[0][1]);
    acc[1][1] = MFMA32(r[d][1], r[d][3], acc[1][1]);
    asm volatile("" : "+v"(acc[0][0]), "+v"(acc[1][0]), "+v"(acc[0][1]), "+v"(acc[1][1]));
    if (s + DEPTH < NS) {
      const int o = (s + DEPTH) * 1024;
      r[d][0] = bld<0>(ra, lo, a0 + o); r[d][2] = bld<AUX>(rb, lo, b0 + o);
      r[d][1] = bld<0>(ra, lo, a1 + o); r[d][3] = bld<AUX>(rb, lo, b1 + o);
    }
    __builtin_amdgcn_sched_barrier(0);
  }
}
DEV void signal(char* ws, int idx) {
  asm volatile("s_waitcnt vmcnt(0)" ::: "memory");
  __syncthreads();
  if (threadIdx.x == 0) __hip_atomic_fetch_add((gu32*)(ws + OFF_CNT + idx * 128), 1u, __ATOMIC_RELAXED, __HIP_MEMORY_SCOPE_AGENT);
}
DEV void await(char* ws, int idx, unsigned n, int w) {
  if (w == 0) {
    while ((unsigned)__builtin_amdgcn_readfirstlane(
               __hip_atomic_load((gu32*)(ws + OFF_CNT + idx * 128), __ATOMIC_RELAXED, __HIP_MEMORY_SCOPE_AGENT)) < n)
      __builtin_amdgcn_s_sleep(4);
  }
  __syncthreads();
}
DEV void zero4(f32x16 (&acc)[2][2]) {
#pragma unroll
  for (int i = 0; i < 2; i++)
#pragma unroll
    for (int j = 0; j < 2; j++)
#pragma unroll
      for (int r = 0; r < 16; r++) acc[i][j][r] = 0.f;
}
constexpr int RP = 1056;
DEV void red_write(char* lds, int w, int lane, const f32x16 (&acc)[2][2]) {
#pragma unroll
  for (int j = 0; j < 2; j++)
#pragma unroll
    for (int i = 0; i < 2; i++)
#pragma unroll
      for (int q = 0; q < 4; q++)
        *(f32x4*)(lds + (((w * 2 + j) * 2 + i) * 4 + q) * RP + lane * 16) =
            (f32x4){acc[i][j][4 * q], acc[i][j][4 * q + 1], acc[i][j][4 * q + 2], acc[i][j][4 * q + 3]};
}
DEV void red_read8(const char* lds, int j, int cg, int tk, float (&o)[8]) {
  f32x4 a = {0, 0, 0, 0}, b = {0, 0, 0, 0};
#pragma unroll
  for (int w = 0; w < 4; w++) {
    const char* p = lds + ((w * 2 + j) * 8 + cg) * RP + tk * 16;
    a += *(const f32x4*)p; b += *(const f32x4*)(p + 512);
  }
#pragma unroll
  for (int e = 0; e < 4; e++) { o[e] = a[e]; o[4 + e] = b[e]; }
}

constexpr int LDS2 = 64 * RP;

DEV void exp8(f32x8& p, const f32x16& s, int o) {
#pragma unroll
  for (int i = 0; i < 8; i++) p[i] = __builtin_amdgcn_exp2f(s[o + i]);
}
DEV float max16(const f32x16& s) {
  float m = fmaxf(s[0], s[1]);
#pragma unroll
  for (int i = 2; i < 16; i += 2) m = fmaxf(m, fmaxf(s[i], s[i + 1]));
  return m;
}
DEV f32x16 splat16(float v) { f32x16 c;
#pragma unroll
  for (int i = 0; i < 16; i++) c[i] = v;
  return c;
}

DEV void attention(char* lds, char* __restrict__ ws, int tbp, int hg) {
  const int t = threadIdx.x, lane = t & 63, w = __builtin_amdgcn_readfirstlane(t >> 6), tb = w >> 1, kh = w & 1;
  const int lo = lane * 16;
  TS(1, 0);
  const rsrc_t krs = mkrsrc(ws + OFF_KF, 65536), vrs = mkrsrc(ws + OFF_VF, VF_BYTES);
  const int m_ = lane & 31;
  const int vo = m_ < 16 ? (lane >> 5) * 256 + m_ * 16 : ((m_ == 16 || m_ == 20) ? 512 : 0x40000000);
#define LDV(g) bld<0>(vrs, vo, (g) * 528)
#define LDK(kb) bld<0>(krs, lo, (kb) * 1024)
  u32x4 q0, q1, q2, q3;
  float m0, m1, m2, m3;
  const int kb0 = kh * 32;
  u32x4 kf[2], va[2], vb[2];
  {
    f32x16 acc[2][2]; zero4(acc);
    u32x4 r[8][4];
    const rsrc_t ra = mkrsrc(ws + OFF_WQ, 512 << 10), rb = mkrsrc(ws + OFF_XN, 2 << 20);
    const int a0 = ((hg * 2 + kh) * 32 + 8 * w) * 1024, b0 = ((tbp * 2 + tb) * 32 + 8 * w) * 1024;
    tileA<8>(r, ra, a0, a0 ^ 32768, lo);
    tile64<8, 8, 0>(r, ra, a0, a0 ^ 32768, rb, b0, b0 ^ 32768, lo, acc);
    const u32x4 k0 = LDK(0), k1 = LDK(1);
    kf[0] = LDK(kb0); va[0] = LDV(2 * kb0); vb[0] = LDV(2 * kb0 + 1);
    kf[1] = LDK(kb0 + 1); va[1] = LDV(2 * kb0 + 2);
    TS(1, 1);
#pragma unroll
    for (int x = 1; x < 4; x++) {
      const f32x16& a = acc[x & 1][x >> 1];
      char* p = lds + (((w ^ x) * 3 + x - 1) * 4) * 1024 + lo;
#pragma unroll
      for (int q = 0; q < 4; q++) *(f32x4*)(p + q * 1024) = (f32x4){a[4 * q], a[4 * q + 1], a[4 * q + 2], a[4 * q + 3]};
    }
    __syncthreads();
    const float sc = 0.25f * 1.4426950408889634f;
    unsigned qq[8];
#pragma unroll
    for (int q = 0; q < 4; q++) {
      const char* p = lds + (w * 12 + q) * 1024 + lo;
      f32x4 a = *(const f32x4*)p + *(const f32x4*)(p + 4096) + *(const f32x4*)(p + 8192);
#pragma unroll
      for (int e = 0; e < 4; e++) a[e] = (a[e] + acc[0][0][4 * q + e]) * sc;
      qq[2 * q] = pk(a[0], a[1]); qq[2 * q + 1] = pk(a[2], a[3]);
    }
    q0 = (u32x4){qq[0], qq[1], qq[2], qq[3]}; q1 = (u32x4){qq[4], qq[5], qq[6], qq[7]};
    const f32x16 z = splat16(0.f);
    m0 = fmaxf(max16(MFMA32(k0, q0, z)), max16(MFMA32(k1, q0, z)));
    m1 = fmaxf(max16(MFMA32(k0, q1, z)), max16(MFMA32(k1, q1, z)));
    m0 = fmaxf(m0, other_half(m0)) + 64.f;
    m1 = fmaxf(m1, other_half(m1)) + 64.f;
    char* xq = lds + 49152;
    *(u32x4*)(xq + (w * 3) * 1024 + lo) = q0;
    *(u32x4*)(xq + (w * 3 + 1) * 1024 + lo) = q1;
    *(f32x2*)(xq + (w * 3 + 2) * 1024 + lane * 8) = (f32x2){m0, m1};
    __syncthreads();
    q2 = *(const u32x4*)(xq + ((w ^ 1) * 3) * 1024 + lo);
    q3 = *(const u32x4*)(xq + ((w ^ 1) * 3 + 1) * 1024 + lo);
    const f32x2 mm = *(const f32x2*)(xq + ((w ^ 1) * 3 + 2) * 1024 + lane * 8);
    m2 = mm[0]; m3 = mm[1];
  }
  f32x16 o0, o1, o2, o3;
  {
    TS(1, 2);
    const f32x16 c0 = splat16(-m0), c1 = splat16(-m1), c2 = splat16(-m2), c3 = splat16(-m3);
    o0 = o1 = o2 = o3 = splat16(0.f);
    f32x16 s0 = MFMA32(kf[0], q0, c0), s1 = MFMA32(kf[0], q1, c1), s2 = MFMA32(kf[0], q2, c2), s3 = splat16(0.f);
    u32x4 vp = vb[0];
#define PIN(x) do { asm volatile("" : "+v"(x)); __builtin_amdgcn_sched_barrier(0); } while (0)
#define EXPN(s, o, n) do { _Pragma("unroll") for (int i_ = 0; i_ < n; i_++) s[(o) + i_] = __builtin_amdgcn_exp2f(s[(o) + i_]); PIN(s); } while (0)
#define EXP4(s, o) EXPN(s, o, 4)
#define LO8(s) __builtin_shufflevector(s, s, 0, 1, 2, 3, 4, 5, 6, 7)
#define HI8(s) __builtin_shufflevector(s, s, 8, 9, 10, 11, 12, 13, 14, 15)
#define TILE(X, OX, V0, Y, OY, VY, KN, QY, CY) do { \
      EXPN(X, 0, 3); OY = SMF(VY, HI8(Y), OY); PIN(OY); \
      EXPN(X, 3, 6); OX = SMF(V0, LO8(X), OX); PIN(OX); \
      EXPN(X, 9, 5); Y = MFMA32(KN, QY, CY); PIN(Y); \
      EXPN(X, 14, 2); } while (0)
    for (int it = 0; it < 32; it += 2) {
#pragma unroll
      for (int u = 0; u < 2; u++) {
        const int nk = (kb0 + it + u + 2) & 63;
        TILE(s0, o0, va[u], s3, o3, vp, kf[u], q3, c3);
        kf[u] = LDK(nk);
        if (u == 0) vb[1] = LDV(2 * ((nk - 1) & 63) + 1); else vb[0] = LDV(2 * ((nk - 1) & 63) + 1);
        TILE(s1, o1, va[u], s0, o0, vb[u], kf[u ^ 1], q0, c0);
        TILE(s2, o2, va[u], s1, o1, vb[u], kf[u ^ 1], q1, c1);
        TILE(s3, o3, va[u], s2, o2, vb[u], kf[u ^ 1], q2, c2);
        va[u] = LDV(2 * nk);
        vp = vb[u];
      }
    }
    o3 = SMF(vp, HI8(s3), o3);
  }
  TS(1, 3);
  float r[2][9];
  bool second = false;
  for (;;) {
#pragma unroll
    for (int e = 0; e < 2; e++) {
      const f32x16 s = e ? o3 : o2;
      char* p = lds + ((w * 2 + e) * 3) * 1024;
      *(f32x4*)(p + lo) = (f32x4){s[0], s[1], s[2], s[3]};
      *(f32x4*)(p + 1024 + lo) = (f32x4){s[4], s[5], s[6], s[7]};
      *(float*)(p + 2048 + lane * 4) = s[8];
    }
    __syncthreads();
    bool bad = false;
#pragma unroll
    for (int e = 0; e < 2; e++) {
      const f32x16 s = e ? o1 : o0;
      const char* p = lds + (((w ^ 1) * 2 + e) * 3) * 1024;
      const f32x4 a = *(const f32x4*)(p + lo), b = *(const f32x4*)(p + 1024 + lo);
      const float l = s[8] + *(const float*)(p + 2048 + lane * 4);
      float am = 0.f;
#pragma unroll
      for (int i = 0; i < 4; i++) {
        r[e][i] = s[i] + a[i]; r[e][4 + i] = s[4 + i] + b[i];
        am += fabsf(r[e][i]) + fabsf(r[e][4 + i]);
      }
      r[e][8] = l;
      bad |= !(l >= 0x1p-100f && l <= 0x1p120f && am <= 0x1p126f);
    }
    if (second) break;
    int* vote = (int*)(lds + 30720);
    if (lane == 0) vote[w] = 0;
    if (bad) vote[w] = 1;
    __syncthreads();
    if (__builtin_amdgcn_readfirstlane(vote[0] | vote[1] | vote[2] | vote[3]) == 0) break;
    second = true;
    TS(1, 5);
    float* mx = (float*)(lds + 32768);
    for (int hd = 0; hd < 4; hd++) {
      const u32x4 q = hd == 0 ? q0 : hd == 1 ? q1 : hd == 2 ? q2 : q3;
      const f32x16 z = splat16(0.f);
      float m = -3.0e38f;
      for (int kb = kb0; kb < kb0 + 32; kb++) m = fmaxf(m, max16(MFMA32(LDK(kb), q, z)));
      mx[(hd * 4 + w) * 64 + lane] = fmaxf(m, other_half(m));
    }
    __syncthreads();
    for (int hd = 0; hd < 4; hd++) {
      const u32x4 q = hd == 0 ? q0 : hd == 1 ? q1 : hd == 2 ? q2 : q3;
      const float m = fmaxf(mx[(hd * 4 + w) * 64 + lane], mx[((hd ^ 2) * 4 + (w ^ 1)) * 64 + lane]);
      const f32x16 c = splat16(-m);
      f32x16 oo = splat16(0.f);
      for (int kb = kb0; kb < kb0 + 32; kb++) {
        const f32x16 s = MFMA32(LDK(kb), q, c);
        f32x8 p0, p1;
        exp8(p0, s, 0); oo = SMF(LDV(2 * kb), p0, oo);
        exp8(p1, s, 8); oo = SMF(LDV(2 * kb + 1), p1, oo);
      }
      if (hd == 0) o0 = oo; else if (hd == 1) o1 = oo; else if (hd == 2) o2 = oo; else o3 = oo;
    }
  }
  const rsrc_t ors = mkrsrc(ws + OFF_O, 2 << 20);
#pragma unroll
  for (int e = 0; e < 2; e++) {
    const float il = 1.0f / r[e][8];
    const u32x4 v = {pk(r[e][0] * il, r[e][1] * il), pk(r[e][2] * il, r[e][3] * il), pk(r[e][4] * il, r[e][5] * il),
                     pk(r[e][6] * il, r[e][7] * il)};
    __builtin_amdgcn_raw_buffer_store_b128(v, ors, lo + ((tbp * 2 + tb) * 32 + hg * 4 + 2 * kh + e) * 1024, 0, 16);
  }
  signal(ws, 16 + tbp);
  TS(1, 4);
#undef LDV
#undef LDK
}

constexpr int PLANE = 64 * 224, UPOFF = PLANE + 128, STAGE = 2 * PLANE + 128;
DEV void gemm1(char* lds, const u16* __restrict__ wg, const u16* __restrict__ wu, char* __restrict__ ws, int mt, int nt, int pfx) {
  const int t = threadIdx.x, lane = t & 63, w = __builtin_amdgcn_readfirstlane(t >> 6), half = w >> 1, side = w & 1;
  const char* wsrc = (const char*)(half ? wu : wg) + nt * 224;
  int go[7];
#pragma unroll
  for (int i = 0; i < 7; i++) { const int id = (side * 7 + i) * 64 + lane, row = id / 14; go[i] = row * 3584 + (id - row * 14) * 16; }
  char* ldst = lds + half * UPOFF + side * 7168;
  const char* B0 = ws + OFF_XN + (mt * 4 + half * 2 + side) * 32768 + lane * 16;
  const char* B1 = ws + OFF_XN + (mt * 4 + half * 2 + (side ^ 1)) * 32768 + lane * 16;
  const int h = lane >> 5, blk = (lane >> 4) & 1, q = (lane & 15) >> 2, p = lane & 3;
  const int ao = blk * UPOFF + (8 * h + q) * 224 + p * 8;
  const int a3 = ao + 3 * 32, aj = ao + side * 4 * 32;
  f32x16 acc[3][2], acs;
#pragma unroll
  for (int r = 0; r < 16; r++) { acs[r] = 0.f;
#pragma unroll
    for (int i = 0; i < 3; i++) acc[i][0][r] = acc[i][1][r] = 0.f; }
  u32x4 bb[2][4][2];
  auto issue = [&](int c, int st) {
#pragma unroll
    for (int i = 0; i < 7; i++)
      __builtin_amdgcn_global_load_lds((const gu32*)(wsrc + (size_t)c * (64 * 3584) + go[i]),
                                       (lu32*)(ldst + st * STAGE + i * 1024), 16, 0, 0);
#pragma unroll
    for (int s = 0; s < 4; s++) {
      bb[st][s][0] = *(const u32x4*)(B0 + (c * 4 + s) * 1024);
      bb[st][s][1] = *(const u32x4*)(B1 + (c * 4 + s) * 1024);
    }
  };
  auto compute = [&](int st) {
    const char* sb = lds + st * STAGE;
#pragma unroll
    for (int s = 0; s < 4; s++) {
      const char* pa = sb + s * (16 * 224);
      const u32x4 f3 = tr_frag(pa + a3, pa + a3 + 896);
      u32x4 f[3];
#pragma unroll
      for (int i = 0; i < 3; i++) f[i] = tr_frag(pa + aj + i * 32, pa + aj + i * 32 + 896);
      acs = MFMA32(f3, bb[st][s][0], acs);
#pragma unroll
      for (int i = 0; i < 3; i++) {
        acc[i][0] = MFMA32(f[i], bb[st][s][0], acc[i][0]);
        acc[i][1] = MFMA32(f[i], bb[st][s][1], acc[i][1]);
      }
    }
  };
  TS(1, 0);
  issue(0, 0);
  __builtin_amdgcn_s_waitcnt(0x0f70);
  __syncthreads();
  TS(1, 1);
  for (int c = 0; c < 8; c += 2) {
    issue(c + 1, 1);
    if (WARM && c == 0)
  {
    const char* p = wsrc + (size_t)((mt & 7) * 64 + side * 32 + (lane >> 1)) * 3584 + (lane & 1) * 128;
    __builtin_amdgcn_global_load_lds((const gu32*)p, (lu32*)(lds + 2 * STAGE + w * 256), 4, 0, 0);
    __builtin_amdgcn_global_load_lds((const gu32*)(p + 92), (lu32*)(lds + 2 * STAGE + w * 256), 4, 0, 0);
  }
    __builtin_amdgcn_sched_barrier(0);
    compute(0);
    __builtin_amdgcn_s_waitcnt(0x0f70);
    __syncthreads();
    if (c + 2 < 8) issue(c + 2, 0);
    if (c == 2) {
#pragma unroll
      for (int i = 0; i < 3; i++) {
        const int ln = min(i * 256 + t, 575);
        __builtin_amdgcn_global_load_lds((const gu32*)(ws + OFF_WO + pfx * 73728 + ln * 128), (lu32*)(lds + 2 * STAGE + w * 256), 4, 0, 0);
      }
    }
    __builtin_amdgcn_sched_barrier(0);
    compute(1);
    __builtin_amdgcn_s_waitcnt(0x0f70);
    __syncthreads();
  }
  __builtin_amdgcn_s_setprio(2);
  TS(1, 2);
  const rsrc_t hrs = mkrsrc(ws + OFF_H, 7168 << 10);
  auto put = [&](const f32x16& a, int tb, int fb) {
    float hv[8];
#pragma unroll
    for (int r = 0; r < 8; r++) { const float gt = a[r]; hv[r] = BC(float, max(BC(int, gt), 0)) * a[8 + r]; }
    __builtin_amdgcn_raw_buffer_store_b128((u32x4){pk(hv[0], hv[1]), pk(hv[2], hv[3]), pk(hv[4], hv[5]), pk(hv[6], hv[7])}, hrs,
                                           lane * 16 + (tb * 112 + fb) * 1024, 0, 16);
  };
  const int tb0 = mt * 4 + half * 2 + side, tb1 = tb0 ^ 1, fb = nt * 7;
  put(acs, tb0, fb + 3);
#pragma unroll
  for (int i = 0; i < 3; i++) { put(acc[i][0], tb0, fb + side * 4 + i); put(acc[i][1], tb1, fb + side * 4 + i); }
  TS(1, 3);
}

DEV void tail(char* lds, const u16* __restrict__ x, char* __restrict__ ws, u16* __restrict__ out, int mt, int tbp, int ct) {
  const int t = threadIdx.x, lane = t & 63, w = __builtin_amdgcn_readfirstlane(t >> 6), lo = lane * 16;
  const int cg = t & 7, tk = t >> 3;
  f32x16 acc[2][2];
  u32x4 r[8][4];
  float g[2][8];
  {
    const rsrc_t ra = mkrsrc(ws + OFF_WD, 1792 << 10), rb = mkrsrc(ws + OFF_H, 7168 << 10);
    const int a0 = (ct * 2 * 112 + 28 * w) * 1024, b0 = (tbp * 2 * 112 + 28 * w) * 1024;
    signal(ws, mt);
    tileA<8>(r, ra, a0, a0 + 112 * 1024, lo);
    zero4(acc);
    await(ws, tbp >> 1, 16, w);
    TS(1, 4);
    __builtin_amdgcn_s_setprio(0);
    tile64<28, 8, 16>(r, ra, a0, a0 + 112 * 1024, rb, b0, b0 + 112 * 1024, lo, acc);
    __builtin_amdgcn_s_setprio(2);
    TS(1, 5);
  }
  const rsrc_t ra = mkrsrc(ws + OFF_WO, 512 << 10), rb = mkrsrc(ws + OFF_O, 2 << 20);
  const int a0 = (ct * 2 * 32 + 8 * w) * 1024, b0 = (tbp * 2 * 32 + 8 * w) * 1024;
  tileA<8>(r, ra, a0, a0 + 32768, lo);
  u32x4 xv[2];
#pragma unroll
  for (int j = 0; j < 2; j++) xv[j] = *(const u32x4*)(x + (tbp * 64 + j * 32 + tk) * 512 + ct * 64 + cg * 8);
  red_write(lds, w, lane, acc);
  __syncthreads();
#pragma unroll
  for (int j = 0; j < 2; j++) {
    red_read8(lds, j, cg, tk, g[j]);
#pragma unroll
    for (int e = 0; e < 8; e++) g[j][e] = g[j][e] / (1.0f + __expf(-g[j][e]));
  }
  zero4(acc);
  TS(1, 6);
  await(ws, 16 + tbp, 8, w);
  TS(1, 7);
  tile64<8, 8, 16>(r, ra, a0, a0 + 32768, rb, b0, b0 + 32768, lo, acc);
  TS(1, 8);
  red_write(lds, w, lane, acc);
  __syncthreads();
#pragma unroll
  for (int j = 0; j < 2; j++) {
    float s[8], o[8];
    red_read8(lds, j, cg, tk, s);
#pragma unroll
    for (int e = 0; e < 8; e++) o[e] = ((e & 1) ? hi16(xv[j][e >> 1]) : lo16(xv[j][e >> 1])) + s[e] * g[j][e];
    *(u32x4*)(out + (tbp * 64 + j * 32 + tk) * 512 + ct * 64 + cg * 8) =
        (u32x4){pk(o[0], o[1]), pk(o[2], o[3]), pk(o[4], o[5]), pk(o[6], o[7])};
  }
  TS(1, 9);
}

__global__ __launch_bounds__(256, 2) void k2(const u16* __restrict__ x, const u16* __restrict__ wg, const u16* __restrict__ wu,
                                             char* __restrict__ ws, u16* __restrict__ out) {
  __shared__ __attribute__((aligned(16))) char lds[LDS2];
  const int b = blockIdx.x, xc = b & 7, r = (b >> 3) & 31;
  if ((b < 256) == ATT_FIRST) {
    __builtin_amdgcn_s_setprio(1);
    attention(lds, ws, (xc & 1) * 16 + (r & 15), (xc >> 1) * 2 + (r >> 4));
  } else {
    const int mt = (xc & 1) * 8 + (r & 7);
    gemm1(lds, wg, wu, ws, mt, (xc >> 1) * 4 + (r >> 3), r);
    tail(lds, x, ws, out, mt, xc * 4 + (r & 3), r >> 2);
  }
}

extern "C" void kernel_launch(void* const* d_in, const int* in_sizes, int n_in, void* d_out, int out_size, void* d_ws,
                              size_t ws_size, hipStream_t stream) {
  const u16* x = (const u16*)d_in[0];
  char* ws = (char*)d_ws;
  k1<<<304, 256, 0, stream>>>(x, (const u16*)d_in[1], (const u16*)d_in[2], (const u16*)d_in[3], (const u16*)d_in[4],
                              (const u16*)d_in[8], ws);
  k2<<<512, 256, 0, stream>>>(x, (const u16*)d_in[6], (const u16*)d_in[7], ws, (u16*)d_out);
}
```

```cpp
#include <hip/hip_runtime.h>
#include <stdint.h>
#include <type_traits>

typedef unsigned short u16;
typedef __attribute__((ext_vector_type(2))) __bf16 bf16x2;
typedef __attribute__((ext_vector_type(8))) __bf16 bf16x8;
typedef __attribute__((ext_vector_type(16))) __bf16 bf16x16;
typedef __attribute__((ext_vector_type(2))) float f32x2;
typedef __attribute__((ext_vector_type(4))) float f32x4;
typedef __attribute__((ext_vector_type(8))) float f32x8;
typedef __attribute__((ext_vector_type(16))) float f32x16;
typedef __attribute__((ext_vector_type(2))) unsigned u32x2;
typedef __attribute__((ext_vector_type(4))) unsigned u32x4;
typedef __attribute__((ext_vector_type(4))) short s16x4;
typedef __attribute__((address_space(3))) s16x4 lds_s16x4;
typedef __attribute__((address_space(1))) unsigned gu32;
typedef __attribute__((address_space(3))) unsigned lu32;
typedef __amdgpu_buffer_rsrc_t rsrc_t;
#define ATT_FIRST 0
#define WARM 1
#define ONLY 0
#define APRIO 1
#define GPRIO 0
#define SPREAD 1
#define SPACING 4
#define PIN(x) do { asm volatile("" : "+v"(x)); __builtin_amdgcn_sched_barrier(0); } while (0)
#define DEV static __device__ __forceinline__
#define BC(T, x) __builtin_bit_cast(T, x)
#define MFMA32(a, b, c) __builtin_amdgcn_mfma_f32_32x32x16_bf16(BC(bf16x8, a), BC(bf16x8, b), c, 0, 0, 0)
#define MFMA16(a, b, c) __builtin_amdgcn_mfma_f32_16x16x32_bf16(BC(bf16x8, a), BC(bf16x8, b), c, 0, 0, 0)
#define SMF(a, b, c) __builtin_amdgcn_smfmac_f32_32x32x32_bf16(BC(bf16x8, a), BC(bf16x16, b), c, 0xDDDD, 0, 0)

constexpr size_t OFF_XN = 0;
constexpr size_t OFF_KF = 2u << 20;
constexpr size_t OFF_VF = OFF_KF + (64u << 10);
constexpr size_t OFF_WQ = OFF_VF + (128u << 10);
constexpr size_t OFF_WO = OFF_WQ + (512u << 10);
constexpr size_t OFF_WD = OFF_WO + (512u << 10);
constexpr size_t OFF_H = OFF_WD + (1792u << 10);
constexpr size_t OFF_O = OFF_H + (7168u << 10);
constexpr size_t OFF_CNT = OFF_O + (2048u << 10);

DEV unsigned pk(float a, float b) { return BC(unsigned, __builtin_convertvector((f32x2){a, b}, bf16x2)); }
DEV float lo16(unsigned u) { return BC(float, u << 16); }
DEV float hi16(unsigned u) { return BC(float, u & 0xffff0000u); }
DEV u32x2 tr_read(const char* p) { return BC(u32x2, __builtin_amdgcn_ds_read_tr16_b64_v4i16((lds_s16x4*)p)); }
DEV u32x4 tr_frag(const char* p0, const char* p1) {
  u32x2 a = tr_read(p0), b = tr_read(p1);
  return (u32x4){a.x, a.y, b.x, b.y};
}
template <int C> DEV float dpp(float v) {
  return BC(float, __builtin_amdgcn_update_dpp(0, BC(int, v), C, 0xf, 0xf, false));
}
DEV float sum16(float v) {
  v += dpp<0x128>(v); v += dpp<0x124>(v); v += dpp<0x122>(v); v += dpp<0x121>(v);
  return v;
}
DEV float other_half(float v) {
  return BC(float, __builtin_amdgcn_ds_bpermute(((threadIdx.x & 63) ^ 32) << 2, BC(int, v)));
}

__global__ __launch_bounds__(256) void k1(const u16* __restrict__ x, const u16* __restrict__ wq,
                                          const u16* __restrict__ wk, const u16* __restrict__ wv,
                                          const u16* __restrict__ wo, const u16* __restrict__ wd, char* __restrict__ ws) {
  __shared__ __attribute__((aligned(16))) char lds[16384 + 32768 + 8192];
  const int b = blockIdx.x, t = threadIdx.x, lane = t & 63, w = __builtin_amdgcn_readfirstlane(t >> 6);
  if (b < 128) {
    const int tok = t >> 4, c0 = t & 15;
    u32x4 xv[4], kw[4], vw[4];
#pragma unroll
    for (int i = 0; i < 4; i++) xv[i] = *(const u32x4*)(x + (b * 16 + tok) * 512 + (c0 + 16 * i) * 8);
#pragma unroll
    for (int i = 0; i < 4; i++) { kw[i] = ((const u32x4*)wk)[t + 256 * i]; vw[i] = ((const u32x4*)wv)[t + 256 * i]; }
    float f[32];
#pragma unroll
    for (int i = 0; i < 4; i++)
#pragma unroll
      for (int j = 0; j < 4; j++) { f[i * 8 + 2 * j] = lo16(xv[i][j]); f[i * 8 + 2 * j + 1] = hi16(xv[i][j]); }
    float s = 0;
#pragma unroll
    for (int i = 0; i < 32; i++) s += f[i];
    const float mean = sum16(s) * (1.0f / 512);
    float q = 0;
#pragma unroll
    for (int i = 0; i < 32; i++) { f[i] -= mean; q += f[i] * f[i]; }
    const float rstd = 1.0f / sqrtf(sum16(q) * (1.0f / 512) + 1e-5f);
#pragma unroll
    for (int i = 0; i < 4; i++) {
      u32x4 o;
#pragma unroll
      for (int j = 0; j < 4; j++) o[j] = pk(f[i * 8 + 2 * j] * rstd, f[i * 8 + 2 * j + 1] * rstd);
      *(u32x4*)(lds + (c0 + 16 * i) * 256 + ((tok ^ c0) * 16)) = o;
    }
#pragma unroll
    for (int i = 0; i < 4; i++) {
      *(u32x4*)(lds + 16384 + (t + 256 * i) * 16) = kw[i];
      *(u32x4*)(lds + 32768 + (t + 256 * i) * 16) = vw[i];
    }
    __syncthreads();
    const int n = lane & 15, g = lane >> 4;
    f32x4 ak = {0, 0, 0, 0}, av = {0, 0, 0, 0};
    char* xnf = ws + OFF_XN + (b >> 1) * 32768 + ((b & 1) * 16 + n) * 16;
#pragma unroll
    for (int i = 0; i < 4; i++) {
      const int S = 4 * w + i, c = 4 * S + g;
      const u32x4 xf = *(const u32x4*)(lds + c * 256 + ((n ^ (c & 15)) * 16));
      const char* pw = lds + 16384 + (32 * S + 8 * g + (n >> 2)) * 32 + (n & 3) * 8;
      const u32x4 kf = tr_frag(pw, pw + 128), vf = tr_frag(pw + 16384, pw + 16384 + 128);
      ak = MFMA16(kf, xf, ak);
      av = MFMA16(xf, vf, av);
      *(u32x4*)(xnf + c * 512) = xf;
    }
    f32x4* red = (f32x4*)(lds + 49152);
    red[(w * 2 + 0) * 64 + lane] = ak;
    red[(w * 2 + 1) * 64 + lane] = av;
    __syncthreads();
    if (w < 2) {
      const f32x4 r = red[(0 + w) * 64 + lane] + red[(2 + w) * 64 + lane] + red[(4 + w) * 64 + lane] + red[(6 + w) * 64 + lane];
      const u32x2 o = {pk(r[0], r[1]), pk(r[2], r[3])};
      if (w == 0) *(u32x2*)(ws + OFF_KF + ((b >> 1) * 64 + (g & 1) * 32 + (b & 1) * 16 + n) * 16 + (g >> 1) * 8) = o;
      else *(u32x2*)(ws + OFF_VF + b * 512 + (g >> 1) * 256 + n * 16 + (g & 1) * 8) = o;
    } else if (t == 192) {
      if (b < 48) *(unsigned*)(ws + OFF_CNT + b * 128) = 0;
    }
  } else {
    const int sl = b - 128;
    const u16* src; char* dst; int s, ns;
    if (sl < 32) { src = wq; s = sl; ns = 32; dst = ws + OFF_WQ; }
    else if (sl < 64) { src = wo; s = sl - 32; ns = 32; dst = ws + OFF_WO; }
    else { src = wd; s = sl - 64; ns = 112; dst = ws + OFF_WD; }
    u32x4 v[4];
#pragma unroll
    for (int i = 0; i < 4; i++) v[i] = ((const u32x4*)(src + s * 16 * 512))[t + 256 * i];
#pragma unroll
    for (int i = 0; i < 4; i++) *(u32x4*)(lds + (w + 4 * i) * 1088 + lane * 16) = v[i];
    __syncthreads();
    const int h = lane >> 5, blk = (lane >> 4) & 1, q = (lane & 15) >> 2, p = lane & 3;
    const int r0 = sl < 32 ? 8 * h + q : 4 * h + q, dr = sl < 32 ? 4 : 8;
#pragma unroll
    for (int i = 0; i < 4; i++) {
      const int j = 4 * w + i;
      const char* pp = lds + r0 * 1088 + j * 64 + blk * 32 + p * 8;
      *(u32x4*)(dst + (j * ns + s) * 1024 + lane * 16) = tr_frag(pp, pp + dr * 1088);
    }
  }
}

DEV rsrc_t mkrsrc(const char* p, int bytes) { return __builtin_amdgcn_make_buffer_rsrc((void*)p, 0, bytes, 0x00020000); }
template <int AUX> DEV u32x4 bld(rsrc_t r, int vo, int so) { return BC(u32x4, __builtin_amdgcn_raw_buffer_load_b128(r, vo, so, AUX)); }
DEV u32x4 mksrd(const void* p, unsigned bytes) {
  const uint64_t a = (uint64_t)p;
  return (u32x4){(unsigned)a, (unsigned)(a >> 32) & 0xffffu, bytes, 0x00020000u};
}
DEV unsigned lds_addr(const char* p) { return (unsigned)(uintptr_t)(const __attribute__((address_space(3))) char*)p; }
DEV void dma(unsigned dst, int vo, u32x4 srd, int so) {
  asm volatile("s_mov_b32 m0, %0\n\ts_nop 0\n\tbuffer_load_dwordx4 %1, %2, %3 offen lds" ::"s"(dst), "v"(vo), "s"(srd), "s"(so) : "memory");
}
DEV void dma_half(unsigned dst, int vo, u32x4 srd, int so) {
  asm volatile("s_mov_b32 m0, %0\n\ts_mov_b32 exec_hi, 0\n\tbuffer_load_dwordx4 %1, %2, %3 offen lds\n\ts_mov_b32 exec_hi, -1"
               ::"s"(dst), "v"(vo), "s"(srd), "s"(so) : "memory");
}
template <int N> DEV void vm_barrier() {
  asm volatile("s_waitcnt vmcnt(%0)\n\ts_barrier" ::"i"(N) : "memory");
}
template <int DEPTH> DEV void tileA(u32x4 (&r)[DEPTH][4], rsrc_t ra, int a0, int a1, int lo) {
#pragma unroll
  for (int s = 0; s < DEPTH; s++) { r[s][0] = bld<0>(ra, lo, a0 + s * 1024); r[s][1] = bld<0>(ra, lo, a1 + s * 1024); }
}
template <int NS, int DEPTH, int AUX>
DEV void tile64(u32x4 (&r)[DEPTH][4], rsrc_t ra, int a0, int a1, rsrc_t rb, int b0, int b1, int lo, f32x16 (&acc)[2][2]) {
#pragma unroll
  for (int s = 0; s < DEPTH; s++) { r[s][2] = bld<AUX>(rb, lo, b0 + s * 1024); r[s][3] = bld<AUX>(rb, lo, b1 + s * 1024); }
  __builtin_amdgcn_sched_barrier(0);
#pragma unroll
  for (int s = 0; s < NS; s++) {
    const int d = s % DEPTH;
    acc[0][0] = MFMA32(r[d][0], r[d][2], acc[0][0]);
    acc[1][0] = MFMA32(r[d][1], r[d][2], acc[1][0]);
    acc[0][1] = MFMA32(r[d][0], r[d][3], acc[0][1]);
    acc[1][1] = MFMA32(r[d][1], r[d][3], acc[1][1]);
    asm volatile("" : "+v"(acc[0][0]), "+v"(acc[1][0]), "+v"(acc[0][1]), "+v"(acc[1][1]));
    if (s + DEPTH < NS) {
      const int o = (s + DEPTH) * 1024;
      r[d][0] = bld<0>(ra, lo, a0 + o); r[d][2] = bld<AUX>(rb, lo, b0 + o);
      r[d][1] = bld<0>(ra, lo, a1 + o); r[d][3] = bld<AUX>(rb, lo, b1 + o);
    }
    __builtin_amdgcn_sched_barrier(0);
  }
}
DEV void signal(char* ws, int idx) {
  asm volatile("s_waitcnt vmcnt(0)" ::: "memory");
  __syncthreads();
  if (threadIdx.x == 0) __hip_atomic_fetch_add((gu32*)(ws + OFF_CNT + idx * 128), 1u, __ATOMIC_RELAXED, __HIP_MEMORY_SCOPE_AGENT);
}
DEV void await(char* ws, int idx, unsigned n, int w) {
  if (w == 0) {
    while ((unsigned)__builtin_amdgcn_readfirstlane(
               __hip_atomic_load((gu32*)(ws + OFF_CNT + idx * 128), __ATOMIC_RELAXED, __HIP_MEMORY_SCOPE_AGENT)) < n)
      __builtin_amdgcn_s_sleep(4);
  }
  __syncthreads();
}
DEV void zero4(f32x16 (&acc)[2][2]) {
#pragma unroll
  for (int i = 0; i < 2; i++)
#pragma unroll
    for (int j = 0; j < 2; j++)
#pragma unroll
      for (int r = 0; r < 16; r++) acc[i][j][r] = 0.f;
}
constexpr int RP = 1056;
DEV void red_write(char* lds, int w, int lane, const f32x16 (&acc)[2][2]) {
#pragma unroll
  for (int j = 0; j < 2; j++)
#pragma unroll
    for (int i = 0; i < 2; i++)
#pragma unroll
      for (int q = 0; q < 4; q++)
        *(f32x4*)(lds + (((w * 2 + j) * 2 + i) * 4 + q) * RP + lane * 16) =
            (f32x4){acc[i][j][4 * q], acc[i][j][4 * q + 1], acc[i][j][4 * q + 2], acc[i][j][4 * q + 3]};
}
DEV void red_read8(const char* lds, int j, int cg, int tk, float (&o)[8]) {
  f32x4 a = {0, 0, 0, 0}, b = {0, 0, 0, 0};
#pragma unroll
  for (int w = 0; w < 4; w++) {
    const char* p = lds + ((w * 2 + j) * 8 + cg) * RP + tk * 16;
    a += *(const f32x4*)p; b += *(const f32x4*)(p + 512);
  }
#pragma unroll
  for (int e = 0; e < 4; e++) { o[e] = a[e]; o[4 + e] = b[e]; }
}

constexpr int LDS2 = 78848;

DEV void exp8(f32x8& p, const f32x16& s, int o) {
#pragma unroll
  for (int i = 0; i < 8; i++) p[i] = __builtin_amdgcn_exp2f(s[o + i]);
}
DEV float max16(const f32x16& s) {
  float m = fmaxf(s[0], s[1]);
#pragma unroll
  for (int i = 2; i < 16; i += 2) m = fmaxf(m, fmaxf(s[i], s[i + 1]));
  return m;
}
DEV f32x16 splat16(float v) { f32x16 c;
#pragma unroll
  for (int i = 0; i < 16; i++) c[i] = v;
  return c;
}

DEV void attention(char* lds, char* __restrict__ ws, int tbp, int hg) {
  const int t = threadIdx.x, lane = t & 63, w = __builtin_amdgcn_readfirstlane(t >> 6), tb = w >> 1, kh = w & 1;
  const int lo = lane * 16;
  const rsrc_t krs = mkrsrc(ws + OFF_KF, 65536), vrs = mkrsrc(ws + OFF_VF, 65536);
  const int m_ = lane & 31;
  constexpr int VP = 1056, HALF = 4096 + 4 * VP, SLOT = 2 * HALF;
  const u32x4 ksrd = mksrd(ws + OFF_KF, 131072);
  const unsigned ring = lds_addr(lds) + kh * HALF + tb * 4096;
  const int gsrc = tb * 65536 + kh * 32768;
  auto fetch1 = [&](int c, int slot, int i) { dma(ring + slot * SLOT + i * (tb ? VP : 1024), lo, ksrd, gsrc + (c * 4 + i) * 1024); };
  auto fetch = [&](int c, int slot) {
#pragma unroll
    for (int i = 0; i < 4; i++) fetch1(c, slot, i);
  };
  const int pad = (m_ == 16 || m_ == 20) ? 1024 : 1040;
  const char* kp = lds + kh * HALF + lo;
  const char* vp0 = lds + kh * HALF + 4096 + (m_ < 16 ? (lane >> 5) * 256 + m_ * 16 : pad);
  const char* vp1 = lds + kh * HALF + 4096 + (m_ < 16 ? 512 + (lane >> 5) * 256 + m_ * 16 : pad);
  const u32x4 ones = {0x3f803f80u, 0x3f803f80u, 0x3f803f80u, 0x3f803f80u};
  fetch(0, 0);
#define LDK(kb) bld<0>(krs, lo, (kb) * 1024)
  u32x4 q0, q1, q2, q3;
  float m0, m1, m2, m3;
  const int kb0 = kh * 32;
  u32x4 kf, va, vb;
  {
    f32x16 acc[2][2]; zero4(acc);
    u32x4 r[8][4];
    const rsrc_t ra = mkrsrc(ws + OFF_WQ, 512 << 10), rb = mkrsrc(ws + OFF_XN, 2 << 20);
    const int a0 = ((hg * 2 + kh) * 32 + 8 * w) * 1024, b0 = ((tbp * 2 + tb) * 32 + 8 * w) * 1024;
    tileA<8>(r, ra, a0, a0 ^ 32768, lo);
    tile64<8, 8, 0>(r, ra, a0, a0 ^ 32768, rb, b0, b0 ^ 32768, lo, acc);
    const u32x4 k0 = LDK(0), k1 = LDK(1);
#pragma unroll
    for (int x = 1; x < 4; x++) {
      const f32x16& a = acc[x & 1][x >> 1];
      char* p = lds + SLOT + (((w ^ x) * 3 + x - 1) * 4) * 1024 + lo;
#pragma unroll
      for (int q = 0; q < 4; q++) *(f32x4*)(p + q * 1024) = (f32x4){a[4 * q], a[4 * q + 1], a[4 * q + 2], a[4 * q + 3]};
    }
    __syncthreads();
    const float sc = 0.25f * 1.4426950408889634f;
    unsigned qq[8];
#pragma unroll
    for (int q = 0; q < 4; q++) {
      const char* p = lds + SLOT + (w * 12 + q) * 1024 + lo;
      f32x4 a = *(const f32x4*)p + *(const f32x4*)(p + 4096) + *(const f32x4*)(p + 8192);
#pragma unroll
      for (int e = 0; e < 4; e++) a[e] = (a[e] + acc[0][0][4 * q + e]) * sc;
      qq[2 * q] = pk(a[0], a[1]); qq[2 * q + 1] = pk(a[2], a[3]);
    }
    q0 = (u32x4){qq[0], qq[1], qq[2], qq[3]}; q1 = (u32x4){qq[4], qq[5], qq[6], qq[7]};
    const f32x16 z = splat16(0.f);
    m0 = fmaxf(max16(MFMA32(k0, q0, z)), max16(MFMA32(k1, q0, z)));
    m1 = fmaxf(max16(MFMA32(k0, q1, z)), max16(MFMA32(k1, q1, z)));
    m0 = fmaxf(m0, other_half(m0)) + 64.f;
    m1 = fmaxf(m1, other_half(m1)) + 64.f;
    char* xq = lds + 4 * SLOT;
    *(u32x4*)(xq + (w * 3) * 1024 + lo) = q0;
    *(u32x4*)(xq + (w * 3 + 1) * 1024 + lo) = q1;
    *(f32x2*)(xq + (w * 3 + 2) * 1024 + lane * 8) = (f32x2){m0, m1};
    const u32x4 fill = (t & 1) ? (u32x4){0, 0, 0, 0} : ones;
    if (t < 16) *(u32x4*)(lds + (t >> 3) * HALF + 4096 + ((t >> 1) & 3) * VP + 1024 + (t & 1) * 16) = fill;
    asm volatile("s_waitcnt vmcnt(0)" ::: "memory");
    __syncthreads();
    fetch(1, 1); fetch(2, 2);
    if (t < 48) *(u32x4*)(lds + SLOT + (t >> 3) * HALF + 4096 + ((t >> 1) & 3) * VP + 1024 + (t & 1) * 16) = fill;
    kf = *(const u32x4*)kp; va = *(const u32x4*)vp0; vb = va;
    q2 = *(const u32x4*)(xq + ((w ^ 1) * 3) * 1024 + lo);
    q3 = *(const u32x4*)(xq + ((w ^ 1) * 3 + 1) * 1024 + lo);
    const f32x2 mm = *(const f32x2*)(xq + ((w ^ 1) * 3 + 2) * 1024 + lane * 8);
    m2 = mm[0]; m3 = mm[1];
  }
  f32x16 o0, o1, o2, o3;
  {
    const f32x16 c0 = splat16(-m0), c1 = splat16(-m1), c2 = splat16(-m2), c3 = splat16(-m3);
    o0 = o1 = o2 = o3 = splat16(0.f);
    f32x16 s0 = MFMA32(kf, q0, c0), s1 = MFMA32(kf, q1, c1), s2 = MFMA32(kf, q2, c2), s3 = splat16(0.f);
#define EXPN(s, o, n) do { _Pragma("unroll") for (int i_ = 0; i_ < n; i_++) { float e_ = __builtin_amdgcn_exp2f(s[(o) + i_]); PIN(e_); s[(o) + i_] = e_; } } while (0)
#define LO8(s) __builtin_shufflevector(s, s, 0, 1, 2, 3, 4, 5, 6, 7)
#define HI8(s) __builtin_shufflevector(s, s, 8, 9, 10, 11, 12, 13, 14, 15)
#define TILE(X, OX, Y, OY, QY, CY, H0, H1, H2) do { \
      EXPN(X, 0, 3); OY = SMF(vb, HI8(Y), OY); PIN(OY); H0; \
      EXPN(X, 3, 5); Y = MFMA32(kf, QY, CY); PIN(Y); H1; \
      EXPN(X, 8, 5); OX = SMF(va, LO8(X), OX); PIN(OX); H2; \
      EXPN(X, 13, 3); } while (0)
    auto chunk = [&](int c, auto first) {
      const int cur = (c & 3) * SLOT, nxt = ((c + 1) & 3) * SLOT;
#pragma unroll
      for (int i = 0; i < 4; i++) {
        if (i == 3) vm_barrier<first ? 4 : 0>();
        const int o1_ = i < 3 ? cur : nxt, i1 = (i + 1) & 3;
        TILE(s0, o0, s3, o3, q3, c3, vb = *(const u32x4*)(vp1 + cur + i * VP), kf = *(const u32x4*)(kp + o1_ + i1 * 1024),
             if (first && i == 3) fetch1(c + 3, (c + 3) & 3, 0));
        TILE(s1, o1, s0, o0, q0, c0, , , if (first && i == 3) fetch1(c + 3, (c + 3) & 3, 1));
        TILE(s2, o2, s1, o1, q1, c1, , , if (first && i == 3) fetch1(c + 3, (c + 3) & 3, 2));
        TILE(s3, o3, s2, o2, q2, c2, if (first && i == 3) fetch1(c + 3, (c + 3) & 3, 3), , va = *(const u32x4*)(vp0 + o1_ + i1 * VP));
      }
    };
    for (int c = 0; c < 5; c++) chunk(c, std::true_type{});
    for (int c = 5; c < 8; c++) chunk(c, std::false_type{});
    o3 = SMF(vb, HI8(s3), o3);
  }
  float r[2][9];
  bool second = false;
  for (;;) {
#pragma unroll
    for (int e = 0; e < 2; e++) {
      const f32x16 s = e ? o3 : o2;
      char* p = lds + ((w * 2 + e) * 3) * 1024;
      *(f32x4*)(p + lo) = (f32x4){s[0], s[1], s[2], s[3]};
      *(f32x4*)(p + 1024 + lo) = (f32x4){s[4], s[5], s[6], s[7]};
      *(float*)(p + 2048 + lane * 4) = s[8];
    }
    __syncthreads();
    bool bad = false;
#pragma unroll
    for (int e = 0; e < 2; e++) {
      const f32x16 s = e ? o1 : o0;
      const char* p = lds + (((w ^ 1) * 2 + e) * 3) * 1024;
      const f32x4 a = *(const f32x4*)(p + lo), b = *(const f32x4*)(p + 1024 + lo);
      const float l = s[8] + *(const float*)(p + 2048 + lane * 4);
      float am = 0.f;
#pragma unroll
      for (int i = 0; i < 4; i++) {
        r[e][i] = s[i] + a[i]; r[e][4 + i] = s[4 + i] + b[i];
        am += fabsf(r[e][i]) + fabsf(r[e][4 + i]);
      }
      r[e][8] = l;
      bad |= !(l >= 0x1p-100f && l <= 0x1p120f && am <= 0x1p126f);
    }
    if (second) break;
    int* vote = (int*)(lds + 30720);
    if (lane == 0) vote[w] = 0;
    if (bad) vote[w] = 1;
    __syncthreads();
    if (__builtin_amdgcn_readfirstlane(vote[0] | vote[1] | vote[2] | vote[3]) == 0) break;
    second = true;
    float* mx = (float*)(lds + 32768);
    for (int hd = 0; hd < 4; hd++) {
      const u32x4 q = hd == 0 ? q0 : hd == 1 ? q1 : hd == 2 ? q2 : q3;
      const f32x16 z = splat16(0.f);
      float m = -3.0e38f;
      for (int kb = kb0; kb < kb0 + 32; kb++) m = fmaxf(m, max16(MFMA32(LDK(kb), q, z)));
      mx[(hd * 4 + w) * 64 + lane] = fmaxf(m, other_half(m));
    }
    __syncthreads();
    for (int hd = 0; hd < 4; hd++) {
      const u32x4 q = hd == 0 ? q0 : hd == 1 ? q1 : hd == 2 ? q2 : q3;
      const float m = fmaxf(mx[(hd * 4 + w) * 64 + lane], mx[((hd ^ 2) * 4 + (w ^ 1)) * 64 + lane]);
      const f32x16 c = splat16(-m);
      f32x16 oo = splat16(0.f);
      for (int kb = kb0; kb < kb0 + 32; kb++) {
        const f32x16 s = MFMA32(LDK(kb), q, c);
        f32x8 p0, p1;
        const int vo = (lane >> 5) * 256 + (m_ & 15) * 16;
        const u32x4 v0 = bld<0>(vrs, vo, kb * 1024), v1 = bld<0>(vrs, vo, kb * 1024 + 512);
        exp8(p0, s, 0); oo = SMF(m_ < 16 ? v0 : ones, p0, oo);
        exp8(p1, s, 8); oo = SMF(m_ < 16 ? v1 : ones, p1, oo);
      }
      if (hd == 0) o0 = oo; else if (hd == 1) o1 = oo; else if (hd == 2) o2 = oo; else o3 = oo;
    }
  }
  const rsrc_t ors = mkrsrc(ws + OFF_O, 2 << 20);
#pragma unroll
  for (int e = 0; e < 2; e++) {
    const float il = 1.0f / r[e][8];
    const u32x4 v = {pk(r[e][0] * il, r[e][1] * il), pk(r[e][2] * il, r[e][3] * il), pk(r[e][4] * il, r[e][5] * il),
                     pk(r[e][6] * il, r[e][7] * il)};
    __builtin_amdgcn_raw_buffer_store_b128(v, ors, lo + ((tbp * 2 + tb) * 32 + hg * 4 + 2 * kh + e) * 1024, 0, 16);
  }
  signal(ws, 16 + tbp);
#undef LDK
}

constexpr int UPOFF = 32 * 224 + 128, STAGE = 2 * 32 * 224 + 128;
DEV void gemm1(char* lds, const u16* __restrict__ wg, const u16* __restrict__ wu, char* __restrict__ ws, int mt, int nt, int pfx) {
  const int t = threadIdx.x, lane = t & 63, w = __builtin_amdgcn_readfirstlane(t >> 6);
  const u32x4 wsrd = mksrd((w & 2) ? wu : wg, 512 * 3584), xsrd = mksrd(ws + OFF_XN, 2 << 20);
  int go[4];
#pragma unroll
  for (int i = 0; i < 4; i++) { const int id = i * 64 + lane, row = id / 14; go[i] = row * 3584 + (id - row * 14) * 16; }
  const int wsrc = nt * 224 + (w & 1) * 16 * 3584, lo = lane * 16, tb = mt * 4 + w;
  const unsigned ldst = lds_addr(lds) + (w >> 1) * UPOFF + (w & 1) * 3584;
  const int h = lane >> 5, blk = (lane >> 4) & 1, q = (lane & 15) >> 2, p = lane & 3;
  const char* pa = lds + blk * UPOFF + (8 * h + q) * 224 + p * 8;
  f32x16 acc[7];
#pragma unroll
  for (int i = 0; i < 7; i++) acc[i] = splat16(0.f);
  u32x4 bb[5][2], f[7];
  auto issue1 = [&](int c, int i) {
    const int st = c % 5;
    if (i < 3) dma(ldst + st * STAGE + i * 1024, go[i], wsrd, wsrc + c * (32 * 3584));
    else if (i == 3) dma_half(ldst + st * STAGE + 3072, go[3], wsrd, wsrc + c * (32 * 3584));
    else if (i < 6)
      asm volatile("buffer_load_dwordx4 %0, %1, %2, %3 offen" : "=v"(bb[st][i - 4]) : "v"(lo), "s"(xsrd), "s"(tb * 32768 + (c * 2 + i - 4) * 1024) : "memory");
  };
  auto issue = [&](int c) {
#pragma unroll
    for (int i = 0; i < 6; i++) issue1(c, i);
  };
  auto rd = [&](int c, int s, int j) {
    const char* pp = pa + (c % 5) * STAGE + s * 3584 + j * 32;
    return tr_frag(pp, pp + 896);
  };
#pragma unroll
  for (int c = 0; c < 4; c++) issue(c);
  vm_barrier<18>();
  asm volatile("" : "+v"(bb[0][0]), "+v"(bb[0][1]));
#pragma unroll
  for (int j = 0; j < 7; j++) f[j] = rd(0, 0, j);
#pragma unroll
  for (int c = 0; c < 16; c++) {
#pragma unroll
    for (int j = 0; j < 7; j++) {
      acc[j] = MFMA32(f[j], bb[c % 5][0], acc[j]); PIN(acc[j]);
      f[j] = rd(c, 1, j);
    }
    if (c < 15) {
      if (c < 13) vm_barrier<12>(); else if (c == 13) vm_barrier<6>(); else vm_barrier<0>();
      asm volatile("" : "+v"(bb[(c + 1) % 5][0]), "+v"(bb[(c + 1) % 5][1]));
      if (!SPREAD && c < 12) issue(c + 4);
      __builtin_amdgcn_sched_barrier(0);
    }
#pragma unroll
    for (int j = 0; j < 7; j++) {
      acc[j] = MFMA32(f[j], bb[c % 5][1], acc[j]); PIN(acc[j]);
      if (c < 15) f[j] = rd(c + 1, 0, j);
      if (SPREAD && c < 12) { issue1(c + 4, j); __builtin_amdgcn_sched_barrier(0); }
    }
    if ((c & 3) == 3) { }
  }
  __builtin_amdgcn_s_setprio(2);
  const rsrc_t hrs = mkrsrc(ws + OFF_H, 7168 << 10);
  auto put = [&](const f32x16& a, int tb, int fb) {
    float hv[8];
#pragma unroll
    for (int r = 0; r < 8; r++) { const float gt = a[r]; hv[r] = BC(float, max(BC(int, gt), 0)) * a[8 + r]; }
    __builtin_amdgcn_raw_buffer_store_b128((u32x4){pk(hv[0], hv[1]), pk(hv[2], hv[3]), pk(hv[4], hv[5]), pk(hv[6], hv[7])}, hrs,
                                           lane * 16 + (tb * 112 + fb) * 1024, 0, 16);
  };
#pragma unroll
  for (int j = 0; j < 7; j++) put(acc[j], tb, nt * 7 + j);
}

DEV void tail(char* lds, const u16* __restrict__ x, char* __restrict__ ws, u16* __restrict__ out, int mt, int tbp, int ct) {
  const int t = threadIdx.x, lane = t & 63, w = __builtin_amdgcn_readfirstlane(t >> 6), lo = lane * 16;
  const int cg = t & 7, tk = t >> 3;
  f32x16 acc[2][2];
  u32x4 r[8][4];
  float g[2][8];
  {
    const rsrc_t ra = mkrsrc(ws + OFF_WD, 1792 << 10), rb = mkrsrc(ws + OFF_H, 7168 << 10);
    const int a0 = (ct * 2 * 112 + 28 * w) * 1024, b0 = (tbp * 2 * 112 + 28 * w) * 1024;
    signal(ws, mt);
    tileA<8>(r, ra, a0, a0 + 112 * 1024, lo);
    zero4(acc);
    await(ws, tbp >> 1, 16, w);
    __builtin_amdgcn_s_setprio(GPRIO);
    tile64<28, 8, 16>(r, ra, a0, a0 + 112 * 1024, rb, b0, b0 + 112 * 1024, lo, acc);
    __builtin_amdgcn_s_setprio(2);
  }
  const rsrc_t ra = mkrsrc(ws + OFF_WO, 512 << 10), rb = mkrsrc(ws + OFF_O, 2 << 20);
  const int a0 = (ct * 2 * 32 + 8 * w) * 1024, b0 = (tbp * 2 * 32 + 8 * w) * 1024;
  tileA<8>(r, ra, a0, a0 + 32768, lo);
  u32x4 xv[2];
#pragma unroll
  for (int j = 0; j < 2; j++) xv[j] = *(const u32x4*)(x + (tbp * 64 + j * 32 + tk) * 512 + ct * 64 + cg * 8);
  red_write(lds, w, lane, acc);
  __syncthreads();
#pragma unroll
  for (int j = 0; j < 2; j++) {
    red_read8(lds, j, cg, tk, g[j]);
#pragma unroll
    for (int e = 0; e < 8; e++) g[j][e] = g[j][e] / (1.0f + __expf(-g[j][e]));
  }
  zero4(acc);
  await(ws, 16 + tbp, 8, w);
  tile64<8, 8, 16>(r, ra, a0, a0 + 32768, rb, b0, b0 + 32768, lo, acc);
  red_write(lds, w, lane, acc);
  __syncthreads();
#pragma unroll
  for (int j = 0; j < 2; j++) {
    float s[8], o[8];
    red_read8(lds, j, cg, tk, s);
#pragma unroll
    for (int e = 0; e < 8; e++) o[e] = ((e & 1) ? hi16(xv[j][e >> 1]) : lo16(xv[j][e >> 1])) + s[e] * g[j][e];
    *(u32x4*)(out + (tbp * 64 + j * 32 + tk) * 512 + ct * 64 + cg * 8) =
        (u32x4){pk(o[0], o[1]), pk(o[2], o[3]), pk(o[4], o[5]), pk(o[6], o[7])};
  }
}

__global__ __launch_bounds__(256, 2) void k2(const u16* __restrict__ x, const u16* __restrict__ wg, const u16* __restrict__ wu,
                                             char* __restrict__ ws, u16* __restrict__ out) {
  __shared__ __attribute__((aligned(16))) char lds[LDS2];
  const int b = blockIdx.x, xc = b & 7, r = (b >> 3) & 31;
  if ((b < 256) == ATT_FIRST) {
    __builtin_amdgcn_s_setprio(APRIO);
    if (ONLY == 1) { signal(ws, 16 + (xc & 1) * 16 + (r & 15)); return; }
    attention(lds, ws, (xc & 1) * 16 + (r & 15), (xc >> 1) * 2 + (r >> 4));
  } else {
    const int mt = (xc & 1) * 8 + (r & 7);
    if (ONLY == 2) return;
    __builtin_amdgcn_s_setprio(GPRIO);
    gemm1(lds, wg, wu, ws, mt, (xc >> 1) * 4 + (r >> 3), r);
    tail(lds, x, ws, out, mt, xc * 4 + (r & 3), r >> 2);
  }
}

extern "C" void kernel_launch(void* const* d_in, const int* in_sizes, int n_in, void* d_out, int out_size, void* d_ws,
                              size_t ws_size, hipStream_t stream) {
  const u16* x = (const u16*)d_in[0];
  char* ws = (char*)d_ws;
  k1<<<304, 256, 0, stream>>>(x, (const u16*)d_in[1], (const u16*)d_in[2], (const u16*)d_in[3], (const u16*)d_in[4],
                              (const u16*)d_in[8], ws);
  k2<<<512, 256, 0, stream>>>(x, (const u16*)d_in[6], (const u16*)d_in[7], ws, (u16*)d_out);
}
```

```cpp
#include <hip/hip_runtime.h>
#include <stdint.h>

typedef unsigned short u16;
typedef __attribute__((ext_vector_type(2))) __bf16 bf16x2;
typedef __attribute__((ext_vector_type(8))) __bf16 bf16x8;
typedef __attribute__((ext_vector_type(16))) __bf16 bf16x16;
typedef __attribute__((ext_vector_type(2))) float f32x2;
typedef __attribute__((ext_vector_type(4))) float f32x4;
typedef __attribute__((ext_vector_type(8))) float f32x8;
typedef __attribute__((ext_vector_type(16))) float f32x16;
typedef __attribute__((ext_vector_type(2))) unsigned u32x2;
typedef __attribute__((ext_vector_type(4))) unsigned u32x4;
typedef __attribute__((ext_vector_type(4))) short s16x4;
typedef __attribute__((address_space(3))) s16x4 lds_s16x4;
typedef __attribute__((address_space(1))) unsigned gu32;
typedef __attribute__((address_space(3))) unsigned lu32;
typedef __amdgpu_buffer_rsrc_t rsrc_t;
#define ATT_FIRST 0
#define WARM 1
#define SPACING 1
#define DEV static __device__ __forceinline__
#define BC(T, x) __builtin_bit_cast(T, x)
#define MFMA32(a, b, c) __builtin_amdgcn_mfma_f32_32x32x16_bf16(BC(bf16x8, a), BC(bf16x8, b), c, 0, 0, 0)
#define MFMA16(a, b, c) __builtin_amdgcn_mfma_f32_16x16x32_bf16(BC(bf16x8, a), BC(bf16x8, b), c, 0, 0, 0)
#define SMF(a, b, c) __builtin_amdgcn_smfmac_f32_32x32x32_bf16(BC(bf16x8, a), BC(bf16x16, b), c, 0xDDDD, 0, 0)

constexpr size_t OFF_XN = 0;
constexpr size_t OFF_KF = 2u << 20;
constexpr size_t OFF_VF = OFF_KF + (64u << 10);
constexpr size_t OFF_WQ = OFF_VF + (128u << 10);
constexpr size_t OFF_WO = OFF_WQ + (512u << 10);
constexpr size_t OFF_WD = OFF_WO + (512u << 10);
constexpr size_t OFF_H = OFF_WD + (1792u << 10);
constexpr size_t OFF_O = OFF_H + (7168u << 10);
constexpr size_t OFF_CNT = OFF_O + (2048u << 10);
constexpr int VF_BYTES = 128 * 528;

DEV unsigned pk(float a, float b) { return BC(unsigned, __builtin_convertvector((f32x2){a, b}, bf16x2)); }
DEV float lo16(unsigned u) { return BC(float, u << 16); }
DEV float hi16(unsigned u) { return BC(float, u & 0xffff0000u); }
DEV u32x2 tr_read(const char* p) { return BC(u32x2, __builtin_amdgcn_ds_read_tr16_b64_v4i16((lds_s16x4*)p)); }
DEV u32x4 tr_frag(const char* p0, const char* p1) {
  u32x2 a = tr_read(p0), b = tr_read(p1);
  return (u32x4){a.x, a.y, b.x, b.y};
}
template <int C> DEV float dpp(float v) {
  return BC(float, __builtin_amdgcn_update_dpp(0, BC(int, v), C, 0xf, 0xf, false));
}
DEV float sum16(float v) {
  v += dpp<0x128>(v); v += dpp<0x124>(v); v += dpp<0x122>(v); v += dpp<0x121>(v);
  return v;
}
DEV float other_half(float v) {
  return BC(float, __builtin_amdgcn_ds_bpermute(((threadIdx.x & 63) ^ 32) << 2, BC(int, v)));
}

__global__ __launch_bounds__(256) void k1(const u16* __restrict__ x, const u16* __restrict__ wq,
                                          const u16* __restrict__ wk, const u16* __restrict__ wv,
                                          const u16* __restrict__ wo, const u16* __restrict__ wd, char* __restrict__ ws) {
  __shared__ __attribute__((aligned(16))) char lds[16384 + 32768 + 8192];
  const int b = blockIdx.x, t = threadIdx.x, lane = t & 63, w = __builtin_amdgcn_readfirstlane(t >> 6);
  if (b < 128) {
    const int tok = t >> 4, c0 = t & 15;
    u32x4 xv[4], kw[4], vw[4];
#pragma unroll
    for (int i = 0; i < 4; i++) xv[i] = *(const u32x4*)(x + (b * 16 + tok) * 512 + (c0 + 16 * i) * 8);
#pragma unroll
    for (int i = 0; i < 4; i++) { kw[i] = ((const u32x4*)wk)[t + 256 * i]; vw[i] = ((const u32x4*)wv)[t + 256 * i]; }
    float f[32];
#pragma unroll
    for (int i = 0; i < 4; i++)
#pragma unroll
      for (int j = 0; j < 4; j++) { f[i * 8 + 2 * j] = lo16(xv[i][j]); f[i * 8 + 2 * j + 1] = hi16(xv[i][j]); }
    float s = 0;
#pragma unroll
    for (int i = 0; i < 32; i++) s += f[i];
    const float mean = sum16(s) * (1.0f / 512);
    float q = 0;
#pragma unroll
    for (int i = 0; i < 32; i++) { f[i] -= mean; q += f[i] * f[i]; }
    const float rstd = 1.0f / sqrtf(sum16(q) * (1.0f / 512) + 1e-5f);
#pragma unroll
    for (int i = 0; i < 4; i++) {
      u32x4 o;
#pragma unroll
      for (int j = 0; j < 4; j++) o[j] = pk(f[i * 8 + 2 * j] * rstd, f[i * 8 + 2 * j + 1] * rstd);
      *(u32x4*)(lds + (c0 + 16 * i) * 256 + ((tok ^ c0) * 16)) = o;
    }
#pragma unroll
    for (int i = 0; i < 4; i++) {
      *(u32x4*)(lds + 16384 + (t + 256 * i) * 16) = kw[i];
      *(u32x4*)(lds + 32768 + (t + 256 * i) * 16) = vw[i];
    }
    __syncthreads();
    const int n = lane & 15, g = lane >> 4;
    f32x4 ak = {0, 0, 0, 0}, av = {0, 0, 0, 0};
    char* xnf = ws + OFF_XN + (b >> 1) * 32768 + ((b & 1) * 16 + n) * 16;
#pragma unroll
    for (int i = 0; i < 4; i++) {
      const int S = 4 * w + i, c = 4 * S + g;
      const u32x4 xf = *(const u32x4*)(lds + c * 256 + ((n ^ (c & 15)) * 16));
      const char* pw = lds + 16384 + (32 * S + 8 * g + (n >> 2)) * 32 + (n & 3) * 8;
      const u32x4 kf = tr_frag(pw, pw + 128), vf = tr_frag(pw + 16384, pw + 16384 + 128);
      ak = MFMA16(kf, xf, ak);
      av = MFMA16(xf, vf, av);
      *(u32x4*)(xnf + c * 512) = xf;
    }
    f32x4* red = (f32x4*)(lds + 49152);
    red[(w * 2 + 0) * 64 + lane] = ak;
    red[(w * 2 + 1) * 64 + lane] = av;
    __syncthreads();
    if (w < 2) {
      const f32x4 r = red[(0 + w) * 64 + lane] + red[(2 + w) * 64 + lane] + red[(4 + w) * 64 + lane] + red[(6 + w) * 64 + lane];
      const u32x2 o = {pk(r[0], r[1]), pk(r[2], r[3])};
      if (w == 0) *(u32x2*)(ws + OFF_KF + ((b >> 1) * 64 + (g & 1) * 32 + (b & 1) * 16 + n) * 16 + (g >> 1) * 8) = o;
      else *(u32x2*)(ws + OFF_VF + b * 528 + (g >> 1) * 256 + n * 16 + (g & 1) * 8) = o;
    } else if (t == 192) {
      if (b < 48) *(unsigned*)(ws + OFF_CNT + b * 128) = 0;
    } else if (t == 128) {
      *(u32x4*)(ws + OFF_VF + b * 528 + 512) = (u32x4){0x3f803f80u, 0x3f803f80u, 0x3f803f80u, 0x3f803f80u};
    }
  } else {
    const int sl = b - 128;
    const u16* src; char* dst; int s, ns;
    if (sl < 32) { src = wq; s = sl; ns = 32; dst = ws + OFF_WQ; }
    else if (sl < 64) { src = wo; s = sl - 32; ns = 32; dst = ws + OFF_WO; }
    else { src = wd; s = sl - 64; ns = 112; dst = ws + OFF_WD; }
    u32x4 v[4];
#pragma unroll
    for (int i = 0; i < 4; i++) v[i] = ((const u32x4*)(src + s * 16 * 512))[t + 256 * i];
#pragma unroll
    for (int i = 0; i < 4; i++) *(u32x4*)(lds + (w + 4 * i) * 1088 + lane * 16) = v[i];
    __syncthreads();
    const int h = lane >> 5, blk = (lane >> 4) & 1, q = (lane & 15) >> 2, p = lane & 3;
    const int r0 = sl < 32 ? 8 * h + q : 4 * h + q, dr = sl < 32 ? 4 : 8;
#pragma unroll
    for (int i = 0; i < 4; i++) {
      const int j = 4 * w + i;
      const char* pp = lds + r0 * 1088 + j * 64 + blk * 32 + p * 8;
      *(u32x4*)(dst + (j * ns + s) * 1024 + lane * 16) = tr_frag(pp, pp + dr * 1088);
    }
  }
}

DEV rsrc_t mkrsrc(const char* p, int bytes) { return __builtin_amdgcn_make_buffer_rsrc((void*)p, 0, bytes, 0x00020000); }
template <int AUX> DEV u32x4 bld(rsrc_t r, int vo, int so) { return BC(u32x4, __builtin_amdgcn_raw_buffer_load_b128(r, vo, so, AUX)); }
template <int DEPTH> DEV void tileA(u32x4 (&r)[DEPTH][4], rsrc_t ra, int a0, int a1, int lo) {
#pragma unroll
  for (int s = 0; s < DEPTH; s++) { r[s][0] = bld<0>(ra, lo, a0 + s * 1024); r[s][1] = bld<0>(ra, lo, a1 + s * 1024); }
}
template <int NS, int DEPTH, int AUX>
DEV void tile64(u32x4 (&r)[DEPTH][4], rsrc_t ra, int a0, int a1, rsrc_t rb, int b0, int b1, int lo, f32x16 (&acc)[2][2]) {
#pragma unroll
  for (int s = 0; s < DEPTH; s++) { r[s][2] = bld<AUX>(rb, lo, b0 + s * 1024); r[s][3] = bld<AUX>(rb, lo, b1 + s * 1024); }
  __builtin_amdgcn_sched_barrier(0);
#pragma unroll
  for (int s = 0; s < NS; s++) {
    const int d = s % DEPTH;
    acc[0][0] = MFMA32(r[d][0], r[d][2], acc[0][0]);
    acc[1][0] = MFMA32(r[d][1], r[d][2], acc[1][0]);
    acc[0][1] = MFMA32(r[d][0], r[d][3], acc[0][1]);
    acc[1][1] = MFMA32(r[d][1], r[d][3], acc[1][1]);
    asm volatile("" : "+v"(acc[0][0]), "+v"(acc[1][0]), "+v"(acc[0][1]), "+v"(acc[1][1]));
    if (s + DEPTH < NS) {
      const int o = (s + DEPTH) * 1024;
      r[d][0] = bld<0>(ra, lo, a0 + o); r[d][2] = bld<AUX>(rb, lo, b0 + o);
      r[d][1] = bld<0>(ra, lo, a1 + o); r[d][3] = bld<AUX>(rb, lo, b1 + o);
    }
    __builtin_amdgcn_sched_barrier(0);
  }
}
DEV void signal(char* ws, int idx) {
  asm volatile("s_waitcnt vmcnt(0)" ::: "memory");
  __syncthreads();
  if (threadIdx.x == 0) __hip_atomic_fetch_add((gu32*)(ws + OFF_CNT + idx * 128), 1u, __ATOMIC_RELAXED, __HIP_MEMORY_SCOPE_AGENT);
}
DEV void await(char* ws, int idx, unsigned n, int w) {
  if (w == 0) {
    while ((unsigned)__builtin_amdgcn_readfirstlane(
               __hip_atomic_load((gu32*)(ws + OFF_CNT + idx * 128), __ATOMIC_RELAXED, __HIP_MEMORY_SCOPE_AGENT)) < n)
      __builtin_amdgcn_s_sleep(4);
  }
  __syncthreads();
}
DEV void zero4(f32x16 (&acc)[2][2]) {
#pragma unroll
  for (int i = 0; i < 2; i++)
#pragma unroll
    for (int j = 0; j < 2; j++)
#pragma unroll
      for (int r = 0; r < 16; r++) acc[i][j][r] = 0.f;
}
constexpr int RP = 1056;
DEV void red_write(char* lds, int w, int lane, const f32x16 (&acc)[2][2]) {
#pragma unroll
  for (int j = 0; j < 2; j++)
#pragma unroll
    for (int i = 0; i < 2; i++)
#pragma unroll
      for (int q = 0; q < 4; q++)
        *(f32x4*)(lds + (((w * 2 + j) * 2 + i) * 4 + q) * RP + lane * 16) =
            (f32x4){acc[i][j][4 * q], acc[i][j][4 * q + 1], acc[i][j][4 * q + 2], acc[i][j][4 * q + 3]};
}
DEV void red_read8(const char* lds, int j, int cg, int tk, float (&o)[8]) {
  f32x4 a = {0, 0, 0, 0}, b = {0, 0, 0, 0};
#pragma unroll
  for (int w = 0; w < 4; w++) {
    const char* p = lds + ((w * 2 + j) * 8 + cg) * RP + tk * 16;
    a += *(const f32x4*)p; b += *(const f32x4*)(p + 512);
  }
#pragma unroll
  for (int e = 0; e < 4; e++) { o[e] = a[e]; o[4 + e] = b[e]; }
}

constexpr int LDS2 = 64 * RP;

DEV void exp8(f32x8& p, const f32x16& s, int o) {
#pragma unroll
  for (int i = 0; i < 8; i++) p[i] = __builtin_amdgcn_exp2f(s[o + i]);
}
DEV float max16(const f32x16& s) {
  float m = fmaxf(s[0], s[1]);
#pragma unroll
  for (int i = 2; i < 16; i += 2) m = fmaxf(m, fmaxf(s[i], s[i + 1]));
  return m;
}
DEV f32x16 splat16(float v) { f32x16 c;
#pragma unroll
  for (int i = 0; i < 16; i++) c[i] = v;
  return c;
}

DEV void attention(char* lds, char* __restrict__ ws, int tbp, int hg) {
  const int t = threadIdx.x, lane = t & 63, w = __builtin_amdgcn_readfirstlane(t >> 6), tb = w >> 1, kh = w & 1;
  const int lo = lane * 16;
  const rsrc_t krs = mkrsrc(ws + OFF_KF, 65536), vrs = mkrsrc(ws + OFF_VF, VF_BYTES);
  const int m_ = lane & 31;
  const int vo = m_ < 16 ? (lane >> 5) * 256 + m_ * 16 : ((m_ == 16 || m_ == 20) ? 512 : 0x40000000);
#define LDV(g) bld<0>(vrs, vo, (g) * 528)
#define LDK(kb) bld<0>(krs, lo, (kb) * 1024)
  u32x4 q0, q1, q2, q3;
  float m0, m1, m2, m3;
  const int kb0 = kh * 32;
  u32x4 kf[2], va[2], vb[2];
  {
    f32x16 acc[2][2]; zero4(acc);
    u32x4 r[8][4];
    const rsrc_t ra = mkrsrc(ws + OFF_WQ, 512 << 10), rb = mkrsrc(ws + OFF_XN, 2 << 20);
    const int a0 = ((hg * 2 + kh) * 32 + 8 * w) * 1024, b0 = ((tbp * 2 + tb) * 32 + 8 * w) * 1024;
    tileA<8>(r, ra, a0, a0 ^ 32768, lo);
    tile64<8, 8, 0>(r, ra, a0, a0 ^ 32768, rb, b0, b0 ^ 32768, lo, acc);
    const u32x4 k0 = LDK(0), k1 = LDK(1);
    kf[0] = LDK(kb0); va[0] = LDV(2 * kb0); vb[0] = LDV(2 * kb0 + 1);
    kf[1] = LDK(kb0 + 1); va[1] = LDV(2 * kb0 + 2);
#pragma unroll
    for (int x = 1; x < 4; x++) {
      const f32x16& a = acc[x & 1][x >> 1];
      char* p = lds + (((w ^ x) * 3 + x - 1) * 4) * 1024 + lo;
#pragma unroll
      for (int q = 0; q < 4; q++) *(f32x4*)(p + q * 1024) = (f32x4){a[4 * q], a[4 * q + 1], a[4 * q + 2], a[4 * q + 3]};
    }
    __syncthreads();
    const float sc = 0.25f * 1.4426950408889634f;
    unsigned qq[8];
#pragma unroll
    for (int q = 0; q < 4; q++) {
      const char* p = lds + (w * 12 + q) * 1024 + lo;
      f32x4 a = *(const f32x4*)p + *(const f32x4*)(p + 4096) + *(const f32x4*)(p + 8192);
#pragma unroll
      for (int e = 0; e < 4; e++) a[e] = (a[e] + acc[0][0][4 * q + e]) * sc;
      qq[2 * q] = pk(a[0], a[1]); qq[2 * q + 1] = pk(a[2], a[3]);
    }
    q0 = (u32x4){qq[0], qq[1], qq[2], qq[3]}; q1 = (u32x4){qq[4], qq[5], qq[6], qq[7]};
    const f32x16 z = splat16(0.f);
    m0 = fmaxf(max16(MFMA32(k0, q0, z)), max16(MFMA32(k1, q0, z)));
    m1 = fmaxf(max16(MFMA32(k0, q1, z)), max16(MFMA32(k1, q1, z)));
    m0 = fmaxf(m0, other_half(m0)) + 64.f;
    m1 = fmaxf(m1, other_half(m1)) + 64.f;
    char* xq = lds + 49152;
    *(u32x4*)(xq + (w * 3) * 1024 + lo) = q0;
    *(u32x4*)(xq + (w * 3 + 1) * 1024 + lo) = q1;
    *(f32x2*)(xq + (w * 3 + 2) * 1024 + lane * 8) = (f32x2){m0, m1};
    __syncthreads();
    q2 = *(const u32x4*)(xq + ((w ^ 1) * 3) * 1024 + lo);
    q3 = *(const u32x4*)(xq + ((w ^ 1) * 3 + 1) * 1024 + lo);
    const f32x2 mm = *(const f32x2*)(xq + ((w ^ 1) * 3 + 2) * 1024 + lane * 8);
    m2 = mm[0]; m3 = mm[1];
  }
  f32x16 o0, o1, o2, o3;
  {
    const f32x16 c0 = splat16(-m0), c1 = splat16(-m1), c2 = splat16(-m2), c3 = splat16(-m3);
    o0 = o1 = o2 = o3 = splat16(0.f);
    f32x16 s0 = MFMA32(kf[0], q0, c0), s1 = MFMA32(kf[0], q1, c1), s2 = MFMA32(kf[0], q2, c2), s3 = splat16(0.f);
    u32x4 vp = vb[0];
#define PIN(x) do { asm volatile("" : "+v"(x)); __builtin_amdgcn_sched_barrier(0); } while (0)
#define EXPN(s, o, n) do { _Pragma("unroll") for (int i_ = 0; i_ < n; i_++) s[(o) + i_] = __builtin_amdgcn_exp2f(s[(o) + i_]); PIN(s); } while (0)
#define EXP4(s, o) EXPN(s, o, 4)
#define LO8(s) __builtin_shufflevector(s, s, 0, 1, 2, 3, 4, 5, 6, 7)
#define HI8(s) __builtin_shufflevector(s, s, 8, 9, 10, 11, 12, 13, 14, 15)
#define TILE(X, OX, V0, Y, OY, VY, KN, QY, CY) do { \
      EXPN(X, 0, 3); OY = SMF(VY, HI8(Y), OY); PIN(OY); \
      EXPN(X, 3, 6); OX = SMF(V0, LO8(X), OX); PIN(OX); \
      EXPN(X, 9, 5); Y = MFMA32(KN, QY, CY); PIN(Y); \
      EXPN(X, 14, 2); } while (0)
    for (int it = 0; it < 32; it += 2) {
#pragma unroll
      for (int u = 0; u < 2; u++) {
        const int nk = (kb0 + it + u + 2) & 63;
        TILE(s0, o0, va[u], s3, o3, vp, kf[u], q3, c3);
        kf[u] = LDK(nk);
        if (u == 0) vb[1] = LDV(2 * ((nk - 1) & 63) + 1); else vb[0] = LDV(2 * ((nk - 1) & 63) + 1);
        TILE(s1, o1, va[u], s0, o0, vb[u], kf[u ^ 1], q0, c0);
        TILE(s2, o2, va[u], s1, o1, vb[u], kf[u ^ 1], q1, c1);
        TILE(s3, o3, va[u], s2, o2, vb[u], kf[u ^ 1], q2, c2);
        va[u] = LDV(2 * nk);
        vp = vb[u];
      }
    }
    o3 = SMF(vp, HI8(s3), o3);
  }
  float r[2][9];
  bool second = false;
  for (;;) {
#pragma unroll
    for (int e = 0; e < 2; e++) {
      const f32x16 s = e ? o3 : o2;
      char* p = lds + ((w * 2 + e) * 3) * 1024;
      *(f32x4*)(p + lo) = (f32x4){s[0], s[1], s[2], s[3]};
      *(f32x4*)(p + 1024 + lo) = (f32x4){s[4], s[5], s[6], s[7]};
      *(float*)(p + 2048 + lane * 4) = s[8];
    }
    __syncthreads();
    bool bad = false;
#pragma unroll
    for (int e = 0; e < 2; e++) {
      const f32x16 s = e ? o1 : o0;
      const char* p = lds + (((w ^ 1) * 2 + e) * 3) * 1024;
      const f32x4 a = *(const f32x4*)(p + lo), b = *(const f32x4*)(p + 1024 + lo);
      const float l = s[8] + *(const float*)(p + 2048 + lane * 4);
      float am = 0.f;
#pragma unroll
      for (int i = 0; i < 4; i++) {
        r[e][i] = s[i] + a[i]; r[e][4 + i] = s[4 + i] + b[i];
        am += fabsf(r[e][i]) + fabsf(r[e][4 + i]);
      }
      r[e][8] = l;
      bad |= !(l >= 0x1p-100f && l <= 0x1p120f && am <= 0x1p126f);
    }
    if (second) break;
    int* vote = (int*)(lds + 30720);
    if (lane == 0) vote[w] = 0;
    if (bad) vote[w] = 1;
    __syncthreads();
    if (__builtin_amdgcn_readfirstlane(vote[0] | vote[1] | vote[2] | vote[3]) == 0) break;
    second = true;
    float* mx = (float*)(lds + 32768);
    for (int hd = 0; hd < 4; hd++) {
      const u32x4 q = hd == 0 ? q0 : hd == 1 ? q1 : hd == 2 ? q2 : q3;
      const f32x16 z = splat16(0.f);
      float m = -3.0e38f;
      for (int kb = kb0; kb < kb0 + 32; kb++) m = fmaxf(m, max16(MFMA32(LDK(kb), q, z)));
      mx[(hd * 4 + w) * 64 + lane] = fmaxf(m, other_half(m));
    }
    __syncthreads();
    for (int hd = 0; hd < 4; hd++) {
      const u32x4 q = hd == 0 ? q0 : hd == 1 ? q1 : hd == 2 ? q2 : q3;
      const float m = fmaxf(mx[(hd * 4 + w) * 64 + lane], mx[((hd ^ 2) * 4 + (w ^ 1)) * 64 + lane]);
      const f32x16 c = splat16(-m);
      f32x16 oo = splat16(0.f);
      for (int kb = kb0; kb < kb0 + 32; kb++) {
        const f32x16 s = MFMA32(LDK(kb), q, c);
        f32x8 p0, p1;
        exp8(p0, s, 0); oo = SMF(LDV(2 * kb), p0, oo);
        exp8(p1, s, 8); oo = SMF(LDV(2 * kb + 1), p1, oo);
      }
      if (hd == 0) o0 = oo; else if (hd == 1) o1 = oo; else if (hd == 2) o2 = oo; else o3 = oo;
    }
  }
  const rsrc_t ors = mkrsrc(ws + OFF_O, 2 << 20);
#pragma unroll
  for (int e = 0; e < 2; e++) {
    const float il = 1.0f / r[e][8];
    const u32x4 v = {pk(r[e][0] * il, r[e][1] * il), pk(r[e][2] * il, r[e][3] * il), pk(r[e][4] * il, r[e][5] * il),
                     pk(r[e][6] * il, r[e][7] * il)};
    __builtin_amdgcn_raw_buffer_store_b128(v, ors, lo + ((tbp * 2 + tb) * 32 + hg * 4 + 2 * kh + e) * 1024, 0, 16);
  }
  signal(ws, 16 + tbp);
#undef LDV
#undef LDK
}

constexpr int PLANE = 64 * 224, UPOFF = PLANE + 128, STAGE = 2 * PLANE + 128;
DEV void gemm1(char* lds, const u16* __restrict__ wg, const u16* __restrict__ wu, char* __restrict__ ws, int mt, int nt, int pfx) {
  const int t = threadIdx.x, lane = t & 63, w = __builtin_amdgcn_readfirstlane(t >> 6), half = w >> 1, side = w & 1;
  const char* wsrc = (const char*)(half ? wu : wg) + nt * 224;
  int go[7];
#pragma unroll
  for (int i = 0; i < 7; i++) { const int id = (side * 7 + i) * 64 + lane, row = id / 14; go[i] = row * 3584 + (id - row * 14) * 16; }
  char* ldst = lds + half * UPOFF + side * 7168;
  const char* B0 = ws + OFF_XN + (mt * 4 + half * 2 + side) * 32768 + lane * 16;
  const char* B1 = ws + OFF_XN + (mt * 4 + half * 2 + (side ^ 1)) * 32768 + lane * 16;
  const int h = lane >> 5, blk = (lane >> 4) & 1, q = (lane & 15) >> 2, p = lane & 3;
  const int ao = blk * UPOFF + (8 * h + q) * 224 + p * 8;
  const int a3 = ao + 3 * 32, aj = ao + side * 4 * 32;
  f32x16 acc[3][2], acs;
#pragma unroll
  for (int r = 0; r < 16; r++) { acs[r] = 0.f;
#pragma unroll
    for (int i = 0; i < 3; i++) acc[i][0][r] = acc[i][1][r] = 0.f; }
  u32x4 bb[2][4][2];
  auto issue = [&](int c, int st) {
#pragma unroll
    for (int i = 0; i < 7; i++)
      __builtin_amdgcn_global_load_lds((const gu32*)(wsrc + (size_t)c * (64 * 3584) + go[i]),
                                       (lu32*)(ldst + st * STAGE + i * 1024), 16, 0, 0);
#pragma unroll
    for (int s = 0; s < 4; s++) {
      bb[st][s][0] = *(const u32x4*)(B0 + (c * 4 + s) * 1024);
      bb[st][s][1] = *(const u32x4*)(B1 + (c * 4 + s) * 1024);
    }
  };
  auto compute = [&](int st) {
    const char* sb = lds + st * STAGE;
#pragma unroll
    for (int s = 0; s < 4; s++) {
      const char* pa = sb + s * (16 * 224);
      const u32x4 f3 = tr_frag(pa + a3, pa + a3 + 896);
      u32x4 f[3];
#pragma unroll
      for (int i = 0; i < 3; i++) f[i] = tr_frag(pa + aj + i * 32, pa + aj + i * 32 + 896);
      acs = MFMA32(f3, bb[st][s][0], acs);
#pragma unroll
      for (int i = 0; i < 3; i++) {
        acc[i][0] = MFMA32(f[i], bb[st][s][0], acc[i][0]);
        acc[i][1] = MFMA32(f[i], bb[st][s][1], acc[i][1]);
      }
    }
  };
  issue(0, 0);
  __builtin_amdgcn_s_waitcnt(0x0f70);
  __syncthreads();
  for (int c = 0; c < 8; c += 2) {
    issue(c + 1, 1);
    if (WARM && c == 0)
  {
    const char* p = wsrc + (size_t)((mt & 7) * 64 + side * 32 + (lane >> 1)) * 3584 + (lane & 1) * 128;
    __builtin_amdgcn_global_load_lds((const gu32*)p, (lu32*)(lds + 2 * STAGE + w * 256), 4, 0, 0);
    __builtin_amdgcn_global_load_lds((const gu32*)(p + 92), (lu32*)(lds + 2 * STAGE + w * 256), 4, 0, 0);
  }
    __builtin_amdgcn_sched_barrier(0);
    compute(0);
    __builtin_amdgcn_s_waitcnt(0x0f70);
    __syncthreads();
    if (c + 2 < 8) issue(c + 2, 0);
    if (c == 2) {
#pragma unroll
      for (int i = 0; i < 3; i++) {
        const int ln = min(i * 256 + t, 575);
        __builtin_amdgcn_global_load_lds((const gu32*)(ws + OFF_WO + pfx * 73728 + ln * 128), (lu32*)(lds + 2 * STAGE + w * 256), 4, 0, 0);
      }
    }
    __builtin_amdgcn_sched_barrier(0);
    compute(1);
    __builtin_amdgcn_s_waitcnt(0x0f70);
    __syncthreads();
  }
  __builtin_amdgcn_s_setprio(2);
  const rsrc_t hrs = mkrsrc(ws + OFF_H, 7168 << 10);
  auto put = [&](const f32x16& a, int tb, int fb) {
    float hv[8];
#pragma unroll
    for (int r = 0; r < 8; r++) { const float gt = a[r]; hv[r] = BC(float, max(BC(int, gt), 0)) * a[8 + r]; }
    __builtin_amdgcn_raw_buffer_store_b128((u32x4){pk(hv[0], hv[1]), pk(hv[2], hv[3]), pk(hv[4], hv[5]), pk(hv[6], hv[7])}, hrs,
                                           lane * 16 + (tb * 112 + fb) * 1024, 0, 16);
  };
  const int tb0 = mt * 4 + half * 2 + side, tb1 = tb0 ^ 1, fb = nt * 7;
  put(acs, tb0, fb + 3);
#pragma unroll
  for (int i = 0; i < 3; i++) { put(acc[i][0], tb0, fb + side * 4 + i); put(acc[i][1], tb1, fb + side * 4 + i); }
}

DEV void tail(char* lds, const u16* __restrict__ x, char* __restrict__ ws, u16* __restrict__ out, int mt, int tbp, int ct) {
  const int t = threadIdx.x, lane = t & 63, w = __builtin_amdgcn_readfirstlane(t >> 6), lo = lane * 16;
  const int cg = t & 7, tk = t >> 3;
  f32x16 acc[2][2];
  u32x4 r[8][4];
  float g[2][8];
  {
    const rsrc_t ra = mkrsrc(ws + OFF_WD, 1792 << 10), rb = mkrsrc(ws + OFF_H, 7168 << 10);
    const int a0 = (ct * 2 * 112 + 28 * w) * 1024, b0 = (tbp * 2 * 112 + 28 * w) * 1024;
    signal(ws, mt);
    tileA<8>(r, ra, a0, a0 + 112 * 1024, lo);
    zero4(acc);
    await(ws, tbp >> 1, 16, w);
    __builtin_amdgcn_s_setprio(0);
    tile64<28, 8, 16>(r, ra, a0, a0 + 112 * 1024, rb, b0, b0 + 112 * 1024, lo, acc);
    __builtin_amdgcn_s_setprio(2);
  }
  const rsrc_t ra = mkrsrc(ws + OFF_WO, 512 << 10), rb = mkrsrc(ws + OFF_O, 2 << 20);
  const int a0 = (ct * 2 * 32 + 8 * w) * 1024, b0 = (tbp * 2 * 32 + 8 * w) * 1024;
  tileA<8>(r, ra, a0, a0 + 32768, lo);
  u32x4 xv[2];
#pragma unroll
  for (int j = 0; j < 2; j++) xv[j] = *(const u32x4*)(x + (tbp * 64 + j * 32 + tk) * 512 + ct * 64 + cg * 8);
  red_write(lds, w, lane, acc);
  __syncthreads();
#pragma unroll
  for (int j = 0; j < 2; j++) {
    red_read8(lds, j, cg, tk, g[j]);
#pragma unroll
    for (int e = 0; e < 8; e++) g[j][e] = g[j][e] / (1.0f + __expf(-g[j][e]));
  }
  zero4(acc);
  await(ws, 16 + tbp, 8, w);
  tile64<8, 8, 16>(r, ra, a0, a0 + 32768, rb, b0, b0 + 32768, lo, acc);
  red_write(lds, w, lane, acc);
  __syncthreads();
#pragma unroll
  for (int j = 0; j < 2; j++) {
    float s[8], o[8];
    red_read8(lds, j, cg, tk, s);
#pragma unroll
    for (int e = 0; e < 8; e++) o[e] = ((e & 1) ? hi16(xv[j][e >> 1]) : lo16(xv[j][e >> 1])) + s[e] * g[j][e];
    *(u32x4*)(out + (tbp * 64 + j * 32 + tk) * 512 + ct * 64 + cg * 8) =
        (u32x4){pk(o[0], o[1]), pk(o[2], o[3]), pk(o[4], o[5]), pk(o[6], o[7])};
  }
}

__global__ __launch_bounds__(256, 2) void k2(const u16* __restrict__ x, const u16* __restrict__ wg, const u16* __restrict__ wu,
                                             char* __restrict__ ws, u16* __restrict__ out) {
  __shared__ __attribute__((aligned(16))) char lds[LDS2];
  const int b = blockIdx.x, xc = b & 7, r = (b >> 3) & 31;
  if ((b < 256) == ATT_FIRST) {
    __builtin_amdgcn_s_setprio(1);
    attention(lds, ws, (xc & 1) * 16 + (r & 15), (xc >> 1) * 2 + (r >> 4));
  } else {
    const int mt = (xc & 1) * 8 + (r & 7);
    gemm1(lds, wg, wu, ws, mt, (xc >> 1) * 4 + (r >> 3), r);
    tail(lds, x, ws, out, mt, xc * 4 + (r & 3), r >> 2);
  }
}

extern "C" void kernel_launch(void* const* d_in, const int* in_sizes, int n_in, void* d_out, int out_size, void* d_ws,
                              size_t ws_size, hipStream_t stream) {
  const u16* x = (const u16*)d_in[0];
  char* ws = (char*)d_ws;
  k1<<<304, 256, 0, stream>>>(x, (const u16*)d_in[1], (const u16*)d_in[2], (const u16*)d_in[3], (const u16*)d_in[4],
                              (const u16*)d_in[8], ws);
  k2<<<512, 256, 0, stream>>>(x, (const u16*)d_in[6], (const u16*)d_in[7], ws, (u16*)d_out);
}
```

```cpp
#include <hip/hip_runtime.h>
#include <stdint.h>
#include <type_traits>

typedef unsigned short u16;
typedef __attribute__((ext_vector_type(2))) __bf16 bf16x2;
typedef __attribute__((ext_vector_type(8))) __bf16 bf16x8;
typedef __attribute__((ext_vector_type(16))) __bf16 bf16x16;
typedef __attribute__((ext_vector_type(2))) float f32x2;
typedef __attribute__((ext_vector_type(4))) float f32x4;
typedef __attribute__((ext_vector_type(8))) float f32x8;
typedef __attribute__((ext_vector_type(16))) float f32x16;
typedef __attribute__((ext_vector_type(2))) unsigned u32x2;
typedef __attribute__((ext_vector_type(4))) unsigned u32x4;
typedef __attribute__((ext_vector_type(4))) short s16x4;
typedef __attribute__((address_space(3))) s16x4 lds_s16x4;
typedef __attribute__((address_space(1))) unsigned gu32;
typedef __attribute__((address_space(3))) unsigned lu32;
typedef __amdgpu_buffer_rsrc_t rsrc_t;
#define WARM 1
#define APRIO 1
#define GPRIO 0
#define SPREAD 1
#define PIN(x) do { asm volatile("" : "+v"(x)); __builtin_amdgcn_sched_barrier(0); } while (0)
#define DEV static __device__ __forceinline__
#define BC(T, x) __builtin_bit_cast(T, x)
#define MFMA32(a, b, c) __builtin_amdgcn_mfma_f32_32x32x16_bf16(BC(bf16x8, a), BC(bf16x8, b), c, 0, 0, 0)
#define MFMA16(a, b, c) __builtin_amdgcn_mfma_f32_16x16x32_bf16(BC(bf16x8, a), BC(bf16x8, b), c, 0, 0, 0)
#define SMF(a, b, c) __builtin_amdgcn_smfmac_f32_32x32x32_bf16(BC(bf16x8, a), BC(bf16x16, b), c, 0xDDDD, 0, 0)

constexpr size_t OFF_XN = 0;
constexpr size_t OFF_KF = 2u << 20;
constexpr size_t OFF_VF = OFF_KF + (64u << 10);
constexpr size_t OFF_WQ = OFF_VF + (128u << 10);
constexpr size_t OFF_WO = OFF_WQ + (512u << 10);
constexpr size_t OFF_WD = OFF_WO + (512u << 10);
constexpr size_t OFF_H = OFF_WD + (1792u << 10);
constexpr size_t OFF_O = OFF_H + (7168u << 10);
constexpr size_t OFF_FLAG = OFF_O + (2048u << 10);
constexpr int FL_LN = 0, FL_WQ = 128, FL_RT = 160, FL_H = 256, FL_O = 512, FL_EPOCH = 768;

DEV unsigned pk(float a, float b) { return BC(unsigned, __builtin_convertvector((f32x2){a, b}, bf16x2)); }
DEV float lo16(unsigned u) { return BC(float, u << 16); }
DEV float hi16(unsigned u) { return BC(float, u & 0xffff0000u); }
DEV u32x2 tr_read(const char* p) { return BC(u32x2, __builtin_amdgcn_ds_read_tr16_b64_v4i16((lds_s16x4*)p)); }
DEV u32x4 tr_frag(const char* p0, const char* p1) {
  u32x2 a = tr_read(p0), b = tr_read(p1);
  return (u32x4){a.x, a.y, b.x, b.y};
}
template <int C> DEV float dpp(float v) {
  return BC(float, __builtin_amdgcn_update_dpp(0, BC(int, v), C, 0xf, 0xf, false));
}
DEV float sum16(float v) {
  v += dpp<0x128>(v); v += dpp<0x124>(v); v += dpp<0x122>(v); v += dpp<0x121>(v);
  return v;
}
DEV float other_half(float v) {
  return BC(float, __builtin_amdgcn_ds_bpermute(((threadIdx.x & 63) ^ 32) << 2, BC(int, v)));
}

DEV rsrc_t mkrsrc(const char* p, int bytes) { return __builtin_amdgcn_make_buffer_rsrc((void*)p, 0, bytes, 0x00020000); }
template <int AUX> DEV u32x4 bld(rsrc_t r, int vo, int so) { return BC(u32x4, __builtin_amdgcn_raw_buffer_load_b128(r, vo, so, AUX)); }
DEV u32x4 mksrd(const void* p, unsigned bytes) {
  const uint64_t a = (uint64_t)p;
  return (u32x4){(unsigned)a, (unsigned)(a >> 32) & 0xffffu, bytes, 0x00020000u};
}
DEV unsigned lds_addr(const char* p) { return (unsigned)(uintptr_t)(const __attribute__((address_space(3))) char*)p; }
DEV void dma(unsigned dst, int vo, u32x4 srd, int so) {
  asm volatile("s_mov_b32 m0, %0\n\ts_nop 0\n\tbuffer_load_dwordx4 %1, %2, %3 offen lds" ::"s"(dst), "v"(vo), "s"(srd), "s"(so) : "memory");
}
DEV void dma_half(unsigned dst, int vo, u32x4 srd, int so) {
  asm volatile("s_mov_b32 m0, %0\n\ts_mov_b32 exec_hi, 0\n\tbuffer_load_dwordx4 %1, %2, %3 offen lds\n\ts_mov_b32 exec_hi, -1"
               ::"s"(dst), "v"(vo), "s"(srd), "s"(so) : "memory");
}
template <int N> DEV void vm_barrier() {
  asm volatile("s_waitcnt vmcnt(%0)\n\ts_barrier" ::"i"(N) : "memory");
}
#define NOINV 0
#define SLP 2
#define SPINMAX 0
DEV void st128(rsrc_t r, int vo, u32x4 v) { __builtin_amdgcn_raw_buffer_store_b128(v, r, vo, 0, 16); }
DEV void st64(rsrc_t r, int vo, u32x2 v) { __builtin_amdgcn_raw_buffer_store_b64(v, r, vo, 0, 16); }
DEV u32x2 next_epoch(char* ws) {
  const u32x2 e = BC(u32x2, __builtin_amdgcn_raw_buffer_load_b64(mkrsrc(ws + OFF_FLAG, 8192), FL_EPOCH * 8, 0, 16));
  const unsigned e0 = e.x, e1 = e.y;
  const uint64_t v = (((uint64_t)(unsigned)__builtin_amdgcn_readfirstlane(e1) << 32) | (unsigned)__builtin_amdgcn_readfirstlane(e0)) + 1;
  return (u32x2){(unsigned)v, (unsigned)(v >> 32)};
}
DEV void signal(char* ws, int idx, u32x2 ep) {
  asm volatile("s_waitcnt vmcnt(0)" ::: "memory");
  __syncthreads();
  if (threadIdx.x == 0) st64(mkrsrc(ws + OFF_FLAG, 8192), idx * 8, ep);
}
DEV bool raised(u32x4 v, u32x2 ep) {
  return __builtin_amdgcn_ballot_w64(v.x != ep.x || v.y != ep.y || v.z != ep.x || v.w != ep.y) == 0;
}
DEV u32x4 flag_ld(u32x4 srd, int vo) {
  u32x4 v;
  asm volatile("buffer_load_dwordx4 %0, %1, %2, 0 offen sc1\n\ts_waitcnt vmcnt(0)" : "=v"(v) : "v"(vo), "s"(srd) : "memory");
  return v;
}
DEV void await(char* ws, int vo, u32x2 ep) {
  const u32x4 srd = mksrd(ws + OFF_FLAG, 8192);
  for (int it = 0; !raised(flag_ld(srd, vo), ep); it++) {
    if (SPINMAX && it > SPINMAX) break;
    __builtin_amdgcn_s_sleep(SLP);
  }
}
DEV void await2(char* ws, int vo0, int vo1, u32x2 ep) {
  const u32x4 srd = mksrd(ws + OFF_FLAG, 8192);
  for (int it = 0;; it++) {
    u32x4 a, b;
    asm volatile("buffer_load_dwordx4 %0, %2, %4, 0 offen sc1\n\tbuffer_load_dwordx4 %1, %3, %4, 0 offen sc1\n\ts_waitcnt vmcnt(0)"
                 : "=&v"(a), "=&v"(b) : "v"(vo0), "v"(vo1), "s"(srd) : "memory");
    if (raised(a, ep) && raised(b, ep)) break;
    if (SPINMAX && it > SPINMAX) break;
    __builtin_amdgcn_s_sleep(SLP);
  }
}

DEV void layernorm(char* lds, const u16* __restrict__ x, const u16* __restrict__ wk, const u16* __restrict__ wv,
                   char* __restrict__ ws, int b) {
  const int t = threadIdx.x, lane = t & 63, w = __builtin_amdgcn_readfirstlane(t >> 6);
  const int tok = t >> 4, c0 = t & 15;
  u32x4 xv[4], kw[4], vw[4];
#pragma unroll
  for (int i = 0; i < 4; i++) xv[i] = *(const u32x4*)(x + (b * 16 + tok) * 512 + (c0 + 16 * i) * 8);
#pragma unroll
  for (int i = 0; i < 4; i++) { kw[i] = ((const u32x4*)wk)[t + 256 * i]; vw[i] = ((const u32x4*)wv)[t + 256 * i]; }
  float f[32];
#pragma unroll
  for (int i = 0; i < 4; i++)
#pragma unroll
    for (int j = 0; j < 4; j++) { f[i * 8 + 2 * j] = lo16(xv[i][j]); f[i * 8 + 2 * j + 1] = hi16(xv[i][j]); }
  float s = 0;
#pragma unroll
  for (int i = 0; i < 32; i++) s += f[i];
  const float mean = sum16(s) * (1.0f / 512);
  float q = 0;
#pragma unroll
  for (int i = 0; i < 32; i++) { f[i] -= mean; q += f[i] * f[i]; }
  const float rstd = 1.0f / sqrtf(sum16(q) * (1.0f / 512) + 1e-5f);
#pragma unroll
  for (int i = 0; i < 4; i++) {
    u32x4 o;
#pragma unroll
    for (int j = 0; j < 4; j++) o[j] = pk(f[i * 8 + 2 * j] * rstd, f[i * 8 + 2 * j + 1] * rstd);
    *(u32x4*)(lds + (c0 + 16 * i) * 256 + ((tok ^ c0) * 16)) = o;
  }
#pragma unroll
  for (int i = 0; i < 4; i++) {
    *(u32x4*)(lds + 16384 + (t + 256 * i) * 16) = kw[i];
    *(u32x4*)(lds + 32768 + (t + 256 * i) * 16) = vw[i];
  }
  __syncthreads();
  const int n = lane & 15, g = lane >> 4;
  f32x4 ak = {0, 0, 0, 0}, av = {0, 0, 0, 0};
  const rsrc_t rs = mkrsrc(ws + OFF_XN, OFF_WQ);
  const int xnf = (b >> 1) * 32768 + ((b & 1) * 16 + n) * 16;
#pragma unroll
  for (int i = 0; i < 4; i++) {
    const int S = 4 * w + i, c = 4 * S + g;
    const u32x4 xf = *(const u32x4*)(lds + c * 256 + ((n ^ (c & 15)) * 16));
    const char* pw = lds + 16384 + (32 * S + 8 * g + (n >> 2)) * 32 + (n & 3) * 8;
    const u32x4 kf = tr_frag(pw, pw + 128), vf = tr_frag(pw + 16384, pw + 16384 + 128);
    ak = MFMA16(kf, xf, ak);
    av = MFMA16(xf, vf, av);
    st128(rs, xnf + c * 512, xf);
  }
  f32x4* red = (f32x4*)(lds + 49152);
  red[(w * 2 + 0) * 64 + lane] = ak;
  red[(w * 2 + 1) * 64 + lane] = av;
  __syncthreads();
  if (w < 2) {
    const f32x4 r = red[(0 + w) * 64 + lane] + red[(2 + w) * 64 + lane] + red[(4 + w) * 64 + lane] + red[(6 + w) * 64 + lane];
    const u32x2 o = {pk(r[0], r[1]), pk(r[2], r[3])};
    st64(rs, w == 0 ? (int)OFF_KF + ((b >> 1) * 64 + (g & 1) * 32 + (b & 1) * 16 + n) * 16 + (g >> 1) * 8
                    : (int)OFF_VF + b * 512 + (g >> 1) * 256 + n * 16 + (g & 1) * 8, o);
  }
}
template <int N> DEV void retile(char* lds, const u16* __restrict__ wq, const u16* __restrict__ wo, const u16* __restrict__ wd,
                                 char* __restrict__ ws, int sl0) {
  const int t = threadIdx.x, lane = t & 63, w = __builtin_amdgcn_readfirstlane(t >> 6);
  u32x4 v[N][4];
#pragma unroll
  for (int e = 0; e < N; e++) {
    const int sl = sl0 + e;
    const u16* src = sl < 32 ? wq + sl * 8192 : sl < 64 ? wo + (sl - 32) * 8192 : wd + (sl - 64) * 8192;
#pragma unroll
    for (int i = 0; i < 4; i++) v[e][i] = ((const u32x4*)src)[t + 256 * i];
  }
#pragma unroll
  for (int e = 0; e < N; e++)
#pragma unroll
    for (int i = 0; i < 4; i++) *(u32x4*)(lds + e * 17408 + (w + 4 * i) * 1088 + lane * 16) = v[e][i];
  __syncthreads();
  const int h = lane >> 5, blk = (lane >> 4) & 1, q = (lane & 15) >> 2, p = lane & 3;
  const int r0 = sl0 < 32 ? 8 * h + q : 4 * h + q, dr = sl0 < 32 ? 4 : 8;
  const rsrc_t rs = mkrsrc(ws + OFF_WQ, 2816 << 10);
#pragma unroll
  for (int e = 0; e < N; e++) {
    const int sl = sl0 + e, ns = sl < 64 ? 32 : 112, base = sl < 32 ? sl : sl < 64 ? 512 + sl - 32 : 1024 + sl - 64;
#pragma unroll
    for (int i = 0; i < 4; i++) {
      const int j = 4 * w + i;
      const char* pp = lds + e * 17408 + r0 * 1088 + j * 64 + blk * 32 + p * 8;
      st128(rs, (j * ns + base) * 1024 + lane * 16, tr_frag(pp, pp + dr * 1088));
    }
  }
}
template <int DEPTH> DEV void tileA(u32x4 (&r)[DEPTH][4], rsrc_t ra, int a0, int a1, int lo) {
#pragma unroll
  for (int s = 0; s < DEPTH; s++) { r[s][0] = bld<16>(ra, lo, a0 + s * 1024); r[s][1] = bld<16>(ra, lo, a1 + s * 1024); }
}
template <int NS, int DEPTH, int AUX>
DEV void tile64(u32x4 (&r)[DEPTH][4], rsrc_t ra, int a0, int a1, rsrc_t rb, int b0, int b1, int lo, f32x16 (&acc)[2][2]) {
#pragma unroll
  for (int s = 0; s < DEPTH; s++) { r[s][2] = bld<AUX>(rb, lo, b0 + s * 1024); r[s][3] = bld<AUX>(rb, lo, b1 + s * 1024); }
  __builtin_amdgcn_sched_barrier(0);
#pragma unroll
  for (int s = 0; s < NS; s++) {
    const int d = s % DEPTH;
    acc[0][0] = MFMA32(r[d][0], r[d][2], acc[0][0]);
    acc[1][0] = MFMA32(r[d][1], r[d][2], acc[1][0]);
    acc[0][1] = MFMA32(r[d][0], r[d][3], acc[0][1]);
    acc[1][1] = MFMA32(r[d][1], r[d][3], acc[1][1]);
    asm volatile("" : "+v"(acc[0][0]), "+v"(acc[1][0]), "+v"(acc[0][1]), "+v"(acc[1][1]));
    if (s + DEPTH < NS) {
      const int o = (s + DEPTH) * 1024;
      r[d][0] = bld<16>(ra, lo, a0 + o); r[d][2] = bld<AUX>(rb, lo, b0 + o);
      r[d][1] = bld<16>(ra, lo, a1 + o); r[d][3] = bld<AUX>(rb, lo, b1 + o);
    }
    __builtin_amdgcn_sched_barrier(0);
  }
}
DEV void zero4(f32x16 (&acc)[2][2]) {
#pragma unroll
  for (int i = 0; i < 2; i++)
#pragma unroll
    for (int j = 0; j < 2; j++)
#pragma unroll
      for (int r = 0; r < 16; r++) acc[i][j][r] = 0.f;
}
constexpr int RP = 1056;
DEV void red_write(char* lds, int w, int lane, const f32x16 (&acc)[2][2]) {
#pragma unroll
  for (int j = 0; j < 2; j++)
#pragma unroll
    for (int i = 0; i < 2; i++)
#pragma unroll
      for (int q = 0; q < 4; q++)
        *(f32x4*)(lds + (((w * 2 + j) * 2 + i) * 4 + q) * RP + lane * 16) =
            (f32x4){acc[i][j][4 * q], acc[i][j][4 * q + 1], acc[i][j][4 * q + 2], acc[i][j][4 * q + 3]};
}
DEV void red_read8(const char* lds, int j, int cg, int tk, float (&o)[8]) {
  f32x4 a = {0, 0, 0, 0}, b = {0, 0, 0, 0};
#pragma unroll
  for (int w = 0; w < 4; w++) {
    const char* p = lds + ((w * 2 + j) * 8 + cg) * RP + tk * 16;
    a += *(const f32x4*)p; b += *(const f32x4*)(p + 512);
  }
#pragma unroll
  for (int e = 0; e < 4; e++) { o[e] = a[e]; o[4 + e] = b[e]; }
}

constexpr int LDS2 = 78848;

DEV void exp8(f32x8& p, const f32x16& s, int o) {
#pragma unroll
  for (int i = 0; i < 8; i++) p[i] = __builtin_amdgcn_exp2f(s[o + i]);
}
DEV float max16(const f32x16& s) {
  float m = fmaxf(s[0], s[1]);
#pragma unroll
  for (int i = 2; i < 16; i += 2) m = fmaxf(m, fmaxf(s[i], s[i + 1]));
  return m;
}
DEV f32x16 splat16(float v) { f32x16 c;
#pragma unroll
  for (int i = 0; i < 16; i++) c[i] = v;
  return c;
}

DEV void attention(char* lds, char* __restrict__ ws, int tbp, int hg, u32x2 ep) {
  const int t = threadIdx.x, lane = t & 63, w = __builtin_amdgcn_readfirstlane(t >> 6), tb = w >> 1, kh = w & 1;
  const int lo = lane * 16;
  const rsrc_t krs = mkrsrc(ws + OFF_KF, 65536), vrs = mkrsrc(ws + OFF_VF, 65536);
  const int m_ = lane & 31;
  constexpr int VP = 1056, HALF = 4096 + 4 * VP, SLOT = 2 * HALF;
  const u32x4 ksrd = mksrd(ws + OFF_KF, 131072);
  const unsigned ring = lds_addr(lds) + kh * HALF + tb * 4096;
  const int gsrc = tb * 65536 + kh * 32768;
  auto fetch1 = [&](int c, int slot, int i) { dma(ring + slot * SLOT + i * (tb ? VP : 1024), lo, ksrd, gsrc + (c * 4 + i) * 1024); };
  auto fetch = [&](int c, int slot) {
#pragma unroll
    for (int i = 0; i < 4; i++) fetch1(c, slot, i);
  };
  const int pad = (m_ == 16 || m_ == 20) ? 1024 : 1040;
  const char* kp = lds + kh * HALF + lo;
  const char* vp0 = lds + kh * HALF + 4096 + (m_ < 16 ? (lane >> 5) * 256 + m_ * 16 : pad);
  const char* vp1 = lds + kh * HALF + 4096 + (m_ < 16 ? 512 + (lane >> 5) * 256 + m_ * 16 : pad);
  const u32x4 ones = {0x3f803f80u, 0x3f803f80u, 0x3f803f80u, 0x3f803f80u};
#define LDK(kb) bld<16>(krs, lo, (kb) * 1024)
  u32x4 q0, q1, q2, q3;
  float m0, m1, m2, m3;
  const int kb0 = kh * 32;
  u32x4 kf, va, vb;
  {
    f32x16 acc[2][2]; zero4(acc);
    u32x4 r[8][4];
    const rsrc_t ra = mkrsrc(ws + OFF_WQ, 512 << 10), rb = mkrsrc(ws + OFF_XN, 2 << 20);
    const int a0 = ((hg * 2 + kh) * 32 + 8 * w) * 1024, b0 = ((tbp * 2 + tb) * 32 + 8 * w) * 1024;
    tileA<8>(r, ra, a0, a0 ^ 32768, lo);
    const rsrc_t kvrs = mkrsrc(ws + OFF_KF, 131072);
    u32x4 c0[4];
#pragma unroll
    for (int i = 0; i < 4; i++) c0[i] = bld<16>(kvrs, lo, gsrc + i * 1024);
    const u32x4 k0 = LDK(0), k1 = LDK(1);
    tile64<8, 8, 16>(r, ra, a0, a0 ^ 32768, rb, b0, b0 ^ 32768, lo, acc);
#pragma unroll
    for (int i = 0; i < 4; i++) *(u32x4*)(lds + kh * HALF + tb * 4096 + i * (tb ? VP : 1024) + lo) = c0[i];
#pragma unroll
    for (int x = 1; x < 4; x++) {
      const f32x16& a = acc[x & 1][x >> 1];
      char* p = lds + SLOT + (((w ^ x) * 3 + x - 1) * 4) * 1024 + lo;
#pragma unroll
      for (int q = 0; q < 4; q++) *(f32x4*)(p + q * 1024) = (f32x4){a[4 * q], a[4 * q + 1], a[4 * q + 2], a[4 * q + 3]};
    }
    __syncthreads();
    const float sc = 0.25f * 1.4426950408889634f;
    unsigned qq[8];
#pragma unroll
    for (int q = 0; q < 4; q++) {
      const char* p = lds + SLOT + (w * 12 + q) * 1024 + lo;
      f32x4 a = *(const f32x4*)p + *(const f32x4*)(p + 4096) + *(const f32x4*)(p + 8192);
#pragma unroll
      for (int e = 0; e < 4; e++) a[e] = (a[e] + acc[0][0][4 * q + e]) * sc;
      qq[2 * q] = pk(a[0], a[1]); qq[2 * q + 1] = pk(a[2], a[3]);
    }
    q0 = (u32x4){qq[0], qq[1], qq[2], qq[3]}; q1 = (u32x4){qq[4], qq[5], qq[6], qq[7]};
    const f32x16 z = splat16(0.f);
    m0 = fmaxf(max16(MFMA32(k0, q0, z)), max16(MFMA32(k1, q0, z)));
    m1 = fmaxf(max16(MFMA32(k0, q1, z)), max16(MFMA32(k1, q1, z)));
    m0 = fmaxf(m0, other_half(m0)) + 64.f;
    m1 = fmaxf(m1, other_half(m1)) + 64.f;
    char* xq = lds + 4 * SLOT;
    *(u32x4*)(xq + (w * 3) * 1024 + lo) = q0;
    *(u32x4*)(xq + (w * 3 + 1) * 1024 + lo) = q1;
    *(f32x2*)(xq + (w * 3 + 2) * 1024 + lane * 8) = (f32x2){m0, m1};
    const u32x4 fill = (t & 1) ? (u32x4){0, 0, 0, 0} : ones;
    if (t < 16) *(u32x4*)(lds + (t >> 3) * HALF + 4096 + ((t >> 1) & 3) * VP + 1024 + (t & 1) * 16) = fill;
    asm volatile("s_waitcnt vmcnt(0)" ::: "memory");
    __syncthreads();
    fetch(1, 1); fetch(2, 2);
    if (t < 48) *(u32x4*)(lds + SLOT + (t >> 3) * HALF + 4096 + ((t >> 1) & 3) * VP + 1024 + (t & 1) * 16) = fill;
    kf = *(const u32x4*)kp; va = *(const u32x4*)vp0; vb = va;
    q2 = *(const u32x4*)(xq + ((w ^ 1) * 3) * 1024 + lo);
    q3 = *(const u32x4*)(xq + ((w ^ 1) * 3 + 1) * 1024 + lo);
    const f32x2 mm = *(const f32x2*)(xq + ((w ^ 1) * 3 + 2) * 1024 + lane * 8);
    m2 = mm[0]; m3 = mm[1];
  }
  f32x16 o0, o1, o2, o3;
  {
    const f32x16 c0 = splat16(-m0), c1 = splat16(-m1), c2 = splat16(-m2), c3 = splat16(-m3);
    o0 = o1 = o2 = o3 = splat16(0.f);
    f32x16 s0 = MFMA32(kf, q0, c0), s1 = MFMA32(kf, q1, c1), s2 = MFMA32(kf, q2, c2), s3 = splat16(0.f);
#define EXPN(s, o, n) do { _Pragma("unroll") for (int i_ = 0; i_ < n; i_++) { float e_ = __builtin_amdgcn_exp2f(s[(o) + i_]); PIN(e_); s[(o) + i_] = e_; } } while (0)
#define LO8(s) __builtin_shufflevector(s, s, 0, 1, 2, 3, 4, 5, 6, 7)
#define HI8(s) __builtin_shufflevector(s, s, 8, 9, 10, 11, 12, 13, 14, 15)
#define TILE(X, OX, Y, OY, QY, CY, H0, H1, H2) do { \
      EXPN(X, 0, 3); OY = SMF(vb, HI8(Y), OY); PIN(OY); H0; \
      EXPN(X, 3, 5); Y = MFMA32(kf, QY, CY); PIN(Y); H1; \
      EXPN(X, 8, 5); OX = SMF(va, LO8(X), OX); PIN(OX); H2; \
      EXPN(X, 13, 3); } while (0)
    auto chunk = [&](int c, auto first) {
      const int cur = (c & 3) * SLOT, nxt = ((c + 1) & 3) * SLOT;
#pragma unroll
      for (int i = 0; i < 4; i++) {
        if (i == 3) vm_barrier<first ? 4 : 0>();
        const int o1_ = i < 3 ? cur : nxt, i1 = (i + 1) & 3;
        TILE(s0, o0, s3, o3, q3, c3, vb = *(const u32x4*)(vp1 + cur + i * VP), kf = *(const u32x4*)(kp + o1_ + i1 * 1024),
             if (first && i == 3) fetch1(c + 3, (c + 3) & 3, 0));
        TILE(s1, o1, s0, o0, q0, c0, , , if (first && i == 3) fetch1(c + 3, (c + 3) & 3, 1));
        TILE(s2, o2, s1, o1, q1, c1, , , if (first && i == 3) fetch1(c + 3, (c + 3) & 3, 2));
        TILE(s3, o3, s2, o2, q2, c2, if (first && i == 3) fetch1(c + 3, (c + 3) & 3, 3), , va = *(const u32x4*)(vp0 + o1_ + i1 * VP));
      }
    };
    for (int c = 0; c < 5; c++) chunk(c, std::true_type{});
    for (int c = 5; c < 8; c++) chunk(c, std::false_type{});
    o3 = SMF(vb, HI8(s3), o3);
  }
  float r[2][9];
  bool second = false;
  for (;;) {
#pragma unroll
    for (int e = 0; e < 2; e++) {
      const f32x16 s = e ? o3 : o2;
      char* p = lds + ((w * 2 + e) * 3) * 1024;
      *(f32x4*)(p + lo) = (f32x4){s[0], s[1], s[2], s[3]};
      *(f32x4*)(p + 1024 + lo) = (f32x4){s[4], s[5], s[6], s[7]};
      *(float*)(p + 2048 + lane * 4) = s[8];
    }
    __syncthreads();
    bool bad = false;
#pragma unroll
    for (int e = 0; e < 2; e++) {
      const f32x16 s = e ? o1 : o0;
      const char* p = lds + (((w ^ 1) * 2 + e) * 3) * 1024;
      const f32x4 a = *(const f32x4*)(p + lo), b = *(const f32x4*)(p + 1024 + lo);
      const float l = s[8] + *(const float*)(p + 2048 + lane * 4);
      float am = 0.f;
#pragma unroll
      for (int i = 0; i < 4; i++) {
        r[e][i] = s[i] + a[i]; r[e][4 + i] = s[4 + i] + b[i];
        am += fabsf(r[e][i]) + fabsf(r[e][4 + i]);
      }
      r[e][8] = l;
      bad |= !(l >= 0x1p-100f && l <= 0x1p120f && am <= 0x1p126f);
    }
    if (second) break;
    int* vote = (int*)(lds + 30720);
    if (lane == 0) vote[w] = 0;
    if (bad) vote[w] = 1;
    __syncthreads();
    if (__builtin_amdgcn_readfirstlane(vote[0] | vote[1] | vote[2] | vote[3]) == 0) break;
    second = true;
    float* mx = (float*)(lds + 32768);
    for (int hd = 0; hd < 4; hd++) {
      const u32x4 q = hd == 0 ? q0 : hd == 1 ? q1 : hd == 2 ? q2 : q3;
      const f32x16 z = splat16(0.f);
      float m = -3.0e38f;
      for (int kb = kb0; kb < kb0 + 32; kb++) m = fmaxf(m, max16(MFMA32(LDK(kb), q, z)));
      mx[(hd * 4 + w) * 64 + lane] = fmaxf(m, other_half(m));
    }
    __syncthreads();
    for (int hd = 0; hd < 4; hd++) {
      const u32x4 q = hd == 0 ? q0 : hd == 1 ? q1 : hd == 2 ? q2 : q3;
      const float m = fmaxf(mx[(hd * 4 + w) * 64 + lane], mx[((hd ^ 2) * 4 + (w ^ 1)) * 64 + lane]);
      const f32x16 c = splat16(-m);
      f32x16 oo = splat16(0.f);
      for (int kb = kb0; kb < kb0 + 32; kb++) {
        const f32x16 s = MFMA32(LDK(kb), q, c);
        f32x8 p0, p1;
        const int vo = (lane >> 5) * 256 + (m_ & 15) * 16;
        const u32x4 v0 = bld<16>(vrs, vo, kb * 1024), v1 = bld<16>(vrs, vo, kb * 1024 + 512);
        exp8(p0, s, 0); oo = SMF(m_ < 16 ? v0 : ones, p0, oo);
        exp8(p1, s, 8); oo = SMF(m_ < 16 ? v1 : ones, p1, oo);
      }
      if (hd == 0) o0 = oo; else if (hd == 1) o1 = oo; else if (hd == 2) o2 = oo; else o3 = oo;
    }
  }
  const rsrc_t ors = mkrsrc(ws + OFF_O, 2 << 20);
#pragma unroll
  for (int e = 0; e < 2; e++) {
    const float il = 1.0f / r[e][8];
    const u32x4 v = {pk(r[e][0] * il, r[e][1] * il), pk(r[e][2] * il, r[e][3] * il), pk(r[e][4] * il, r[e][5] * il),
                     pk(r[e][6] * il, r[e][7] * il)};
    __builtin_amdgcn_raw_buffer_store_b128(v, ors, lo + ((tbp * 2 + tb) * 32 + hg * 4 + 2 * kh + e) * 1024, 0, 16);
  }
  signal(ws, FL_O + tbp * 8 + hg, ep);
#undef LDK
}

constexpr int UPOFF = 32 * 224 + 128, STAGE = 2 * 32 * 224 + 128;
DEV void gemm1(char* lds, const u16* __restrict__ wg, const u16* __restrict__ wu, char* __restrict__ ws, int mt, int nt, int pfx, u32x2& ep) {
  const int t = threadIdx.x, lane = t & 63, w = __builtin_amdgcn_readfirstlane(t >> 6);
  const u32x4 wsrd = mksrd((w & 2) ? wu : wg, 512 * 3584), xsrd = mksrd(ws + OFF_XN, 2 << 20);
  int go[4];
#pragma unroll
  for (int i = 0; i < 4; i++) { const int id = i * 64 + lane, row = id / 14; go[i] = row * 3584 + (id - row * 14) * 16; }
  const int wsrc = nt * 224 + (w & 1) * 16 * 3584, lo = lane * 16, tb = mt * 4 + w;
  const unsigned ldst = lds_addr(lds) + (w >> 1) * UPOFF + (w & 1) * 3584;
  const int h = lane >> 5, blk = (lane >> 4) & 1, q = (lane & 15) >> 2, p = lane & 3;
  const char* pa = lds + blk * UPOFF + (8 * h + q) * 224 + p * 8;
  f32x16 acc[7];
#pragma unroll
  for (int i = 0; i < 7; i++) acc[i] = splat16(0.f);
  u32x4 bb[5][2], f[7];
  auto issue1 = [&](int c, int i) {
    const int st = c % 5;
    if (i < 3) dma(ldst + st * STAGE + i * 1024, go[i], wsrd, wsrc + c * (32 * 3584));
    else if (i == 3) dma_half(ldst + st * STAGE + 3072, go[3], wsrd, wsrc + c * (32 * 3584));
    else if (i < 6)
      asm volatile("buffer_load_dwordx4 %0, %1, %2, %3 offen sc1" : "=v"(bb[st][i - 4]) : "v"(lo), "s"(xsrd), "s"(tb * 32768 + (c * 2 + i - 4) * 1024) : "memory");
  };
  auto issue = [&](int c) {
#pragma unroll
    for (int i = 0; i < 6; i++) issue1(c, i);
  };
  auto rd = [&](int c, int s, int j) {
    const char* pp = pa + (c % 5) * STAGE + s * 3584 + j * 32;
    return tr_frag(pp, pp + 896);
  };
#pragma unroll
  for (int c = 0; c < 4; c++)
#pragma unroll
    for (int i = 0; i < 4; i++) issue1(c, i);
  ep = next_epoch(ws);
  await(ws, (FL_LN + 2 * tb) * 8, ep);
#pragma unroll
  for (int c = 0; c < 4; c++) { issue1(c, 4); issue1(c, 5); }
  const u32x4 fsrd = mksrd(ws + OFF_FLAG, 8192);
  const int rto = FL_RT * 8 + min(lane, 35) * 16;
  u32x4 rtf;
  vm_barrier<6>();
  asm volatile("" : "+v"(bb[0][0]), "+v"(bb[0][1]));
#pragma unroll
  for (int j = 0; j < 7; j++) f[j] = rd(0, 0, j);
#pragma unroll
  for (int c = 0; c < 16; c++) {
#pragma unroll
    for (int j = 0; j < 7; j++) {
      acc[j] = MFMA32(f[j], bb[c % 5][0], acc[j]); PIN(acc[j]);
      f[j] = rd(c, 1, j);
    }
    if (c < 15) {
      if (c == 0) vm_barrier<4>(); else if (c == 1) vm_barrier<8>(); else if (c < 13) vm_barrier<12>(); else if (c == 13) vm_barrier<6>(); else vm_barrier<0>();
      asm volatile("" : "+v"(bb[(c + 1) % 5][0]), "+v"(bb[(c + 1) % 5][1]));
      if (!SPREAD && c < 12) issue(c + 4);
      __builtin_amdgcn_sched_barrier(0);
    }
#pragma unroll
    for (int j = 0; j < 7; j++) {
      acc[j] = MFMA32(f[j], bb[c % 5][1], acc[j]); PIN(acc[j]);
      if (c < 15) f[j] = rd(c + 1, 0, j);
      if (SPREAD && c < 12) { issue1(c + 4, j); __builtin_amdgcn_sched_barrier(0); }
    }
    if (c == 12) asm volatile("buffer_load_dwordx4 %0, %1, %2, 0 offen sc1" : "=v"(rtf) : "v"(rto), "s"(fsrd) : "memory");
    if ((c & 3) == 3) { }
  }
  __builtin_amdgcn_s_setprio(2);
  const rsrc_t hrs = mkrsrc(ws + OFF_H, 7168 << 10);
  auto put = [&](const f32x16& a, int tb, int fb) {
    float hv[8];
#pragma unroll
    for (int r = 0; r < 8; r++) { const float gt = a[r]; hv[r] = BC(float, max(BC(int, gt), 0)) * a[8 + r]; }
    __builtin_amdgcn_raw_buffer_store_b128((u32x4){pk(hv[0], hv[1]), pk(hv[2], hv[3]), pk(hv[4], hv[5]), pk(hv[6], hv[7])}, hrs,
                                           lane * 16 + (tb * 112 + fb) * 1024, 0, 16);
  };
#pragma unroll
  for (int j = 0; j < 7; j++) put(acc[j], tb, nt * 7 + j);
  asm volatile("" : "+v"(rtf));
  if (!raised(rtf, ep)) await(ws, rto, ep);
}

DEV void tail(char* lds, const u16* __restrict__ x, char* __restrict__ ws, u16* __restrict__ out, int hidx, int tbp, int ct, u32x2 ep) {
  const int t = threadIdx.x, lane = t & 63, w = __builtin_amdgcn_readfirstlane(t >> 6), lo = lane * 16;
  const int cg = t & 7, tk = t >> 3;
  f32x16 acc[2][2];
  u32x4 r[8][4];
  float g[2][8];
  {
    const rsrc_t ra = mkrsrc(ws + OFF_WD, 1792 << 10), rb = mkrsrc(ws + OFF_H, 7168 << 10);
    const int a0 = (ct * 2 * 112 + 28 * w) * 1024, b0 = (tbp * 2 * 112 + 28 * w) * 1024;
    signal(ws, FL_H + hidx, ep);
    tileA<8>(r, ra, a0, a0 + 112 * 1024, lo);
    zero4(acc);
    await(ws, (FL_H + (tbp >> 1) * 16 + 4 * w) * 8 + (lane & 1) * 16, ep);
    __builtin_amdgcn_s_setprio(GPRIO);
    tile64<28, 8, 16>(r, ra, a0, a0 + 112 * 1024, rb, b0, b0 + 112 * 1024, lo, acc);
    __builtin_amdgcn_s_setprio(2);
  }
  const rsrc_t ra = mkrsrc(ws + OFF_WO, 512 << 10), rb = mkrsrc(ws + OFF_O, 2 << 20);
  const int a0 = (ct * 2 * 32 + 8 * w) * 1024, b0 = (tbp * 2 * 32 + 8 * w) * 1024;
  tileA<8>(r, ra, a0, a0 + 32768, lo);
  u32x4 xv[2];
#pragma unroll
  for (int j = 0; j < 2; j++) xv[j] = *(const u32x4*)(x + (tbp * 64 + j * 32 + tk) * 512 + ct * 64 + cg * 8);
  red_write(lds, w, lane, acc);
  __syncthreads();
#pragma unroll
  for (int j = 0; j < 2; j++) {
    red_read8(lds, j, cg, tk, g[j]);
#pragma unroll
    for (int e = 0; e < 8; e++) g[j][e] = g[j][e] / (1.0f + __expf(-g[j][e]));
  }
  zero4(acc);
  __syncthreads();
  await(ws, (FL_O + tbp * 8 + 2 * w) * 8, ep);
  tile64<8, 8, 16>(r, ra, a0, a0 + 32768, rb, b0, b0 + 32768, lo, acc);
  red_write(lds, w, lane, acc);
  __syncthreads();
#pragma unroll
  for (int j = 0; j < 2; j++) {
    float s[8], o[8];
    red_read8(lds, j, cg, tk, s);
#pragma unroll
    for (int e = 0; e < 8; e++) o[e] = ((e & 1) ? hi16(xv[j][e >> 1]) : lo16(xv[j][e >> 1])) + s[e] * g[j][e];
    *(u32x4*)(out + (tbp * 64 + j * 32 + tk) * 512 + ct * 64 + cg * 8) =
        (u32x4){pk(o[0], o[1]), pk(o[2], o[3]), pk(o[4], o[5]), pk(o[6], o[7])};
  }
}

__global__ __launch_bounds__(256, 2) void k2(const u16* __restrict__ x, const u16* __restrict__ wq, const u16* __restrict__ wk,
                                             const u16* __restrict__ wv, const u16* __restrict__ wo, const u16* __restrict__ wg,
                                             const u16* __restrict__ wu, const u16* __restrict__ wd, char* __restrict__ ws,
                                             u16* __restrict__ out) {
  __shared__ __attribute__((aligned(16))) char lds[LDS2];
  const int b = blockIdx.x, xc = b & 7, r = (b >> 3) & 31, lane = threadIdx.x & 63;
  if (b < 256) {
    __builtin_amdgcn_s_setprio(APRIO);
    const u32x2 ep0 = BC(u32x2, __builtin_amdgcn_raw_buffer_load_b64(mkrsrc(ws + OFF_FLAG, 8192), FL_EPOCH * 8, 0, 16));
    if (b < 128) layernorm(lds, x, wk, wv, ws, b);
    else if (b < 160) retile<1>(lds, wq, wo, wd, ws, b - 128);
    else if (b < 232) retile<2>(lds, wq, wo, wd, ws, 2 * b - 288);
    const unsigned e0 = ep0.x, e1 = ep0.y;
    const uint64_t e = (((uint64_t)(unsigned)__builtin_amdgcn_readfirstlane(e1) << 32) | (unsigned)__builtin_amdgcn_readfirstlane(e0)) + 1;
    const u32x2 ep = {(unsigned)e, (unsigned)(e >> 32)};
    if (b < 232) signal(ws, b, ep);
    await2(ws, FL_WQ * 8 + (lane & 15) * 16, FL_LN * 8 + lane * 16, ep);
    if (!NOINV && __builtin_amdgcn_readfirstlane(threadIdx.x) == 0) asm volatile("buffer_inv sc1" ::: "memory");
    attention(lds, ws, (xc & 1) * 16 + (r & 15), (xc >> 1) * 2 + (r >> 4), ep);
  } else {
    const int mt = (xc & 1) * 8 + (r & 7), nt = (xc >> 1) * 4 + (r >> 3);
    __builtin_amdgcn_s_setprio(GPRIO);
    u32x2 ep;
    gemm1(lds, wg, wu, ws, mt, nt, r, ep);
    tail(lds, x, ws, out, mt * 16 + nt, xc * 4 + (r & 3), r >> 2, ep);
    if (b == 511 && threadIdx.x < 64) {
#pragma unroll 1
      for (int i = 0; i < 4; i++) await(ws, FL_H * 8 + i * 1024 + lane * 16, ep);
      if (lane == 0) st64(mkrsrc(ws + OFF_FLAG, 8192), FL_EPOCH * 8, ep);
    }
  }
}

extern "C" void kernel_launch(void* const* d_in, const int* in_sizes, int n_in, void* d_out, int out_size, void* d_ws,
                              size_t ws_size, hipStream_t stream) {
  k2<<<512, 256, 0, stream>>>((const u16*)d_in[0], (const u16*)d_in[1], (const u16*)d_in[2], (const u16*)d_in[3],
                              (const u16*)d_in[4], (const u16*)d_in[6], (const u16*)d_in[7], (const u16*)d_in[8], (char*)d_ws,
                              (u16*)d_out);
}
```

```cpp
#include <hip/hip_runtime.h>
#include <stdint.h>
#include <type_traits>

typedef unsigned short u16;
typedef __attribute__((ext_vector_type(2))) __bf16 bf16x2;
typedef __attribute__((ext_vector_type(8))) __bf16 bf16x8;
typedef __attribute__((ext_vector_type(16))) __bf16 bf16x16;
typedef __attribute__((ext_vector_type(2))) float f32x2;
typedef __attribute__((ext_vector_type(4))) float f32x4;
typedef __attribute__((ext_vector_type(8))) float f32x8;
typedef __attribute__((ext_vector_type(16))) float f32x16;
typedef __attribute__((ext_vector_type(2))) unsigned u32x2;
typedef __attribute__((ext_vector_type(4))) unsigned u32x4;
typedef __attribute__((ext_vector_type(4))) short s16x4;
typedef __attribute__((address_space(3))) s16x4 lds_s16x4;
typedef __attribute__((address_space(1))) unsigned gu32;
typedef __attribute__((address_space(3))) unsigned lu32;
typedef __amdgpu_buffer_rsrc_t rsrc_t;
#define WARM 1
#define APRIO 1
#define GPRIO 0
#define SPREAD 1
#define PIN(x) do { asm volatile("" : "+v"(x)); __builtin_amdgcn_sched_barrier(0); } while (0)
#define DEV static __device__ __forceinline__
#define BC(T, x) __builtin_bit_cast(T, x)
#define MFMA32(a, b, c) __builtin_amdgcn_mfma_f32_32x32x16_bf16(BC(bf16x8, a), BC(bf16x8, b), c, 0, 0, 0)
#define MFMA16(a, b, c) __builtin_amdgcn_mfma_f32_16x16x32_bf16(BC(bf16x8, a), BC(bf16x8, b), c, 0, 0, 0)
#define SMF(a, b, c) __builtin_amdgcn_smfmac_f32_32x32x32_bf16(BC(bf16x8, a), BC(bf16x16, b), c, 0xDDDD, 0, 0)

constexpr size_t OFF_XN = 0;
constexpr size_t OFF_KF = 2u << 20;
constexpr size_t OFF_VF = OFF_KF + (64u << 10);
constexpr size_t OFF_WO = OFF_VF + (64u << 10);
constexpr size_t OFF_WD = OFF_WO + (512u << 10);
constexpr size_t OFF_H = OFF_WD + (1792u << 10);
constexpr size_t OFF_O = OFF_H + (7168u << 10);
constexpr size_t OFF_FLAG = OFF_O + (2048u << 10);
constexpr int FL_LN = 0, FL_RT = 8192, FL_H = 9216, FL_O = FL_H + 8192, FL_EPOCH = FL_O + 16384, FL_BYTES = FL_EPOCH + 128;

DEV unsigned pk(float a, float b) { return BC(unsigned, __builtin_convertvector((f32x2){a, b}, bf16x2)); }
DEV float lo16(unsigned u) { return BC(float, u << 16); }
DEV float hi16(unsigned u) { return BC(float, u & 0xffff0000u); }
DEV u32x2 tr_read(const char* p) { return BC(u32x2, __builtin_amdgcn_ds_read_tr16_b64_v4i16((lds_s16x4*)p)); }
DEV u32x4 tr_frag(const char* p0, const char* p1) {
  u32x2 a = tr_read(p0), b = tr_read(p1);
  return (u32x4){a.x, a.y, b.x, b.y};
}
template <int C> DEV float dpp(float v) {
  return BC(float, __builtin_amdgcn_update_dpp(0, BC(int, v), C, 0xf, 0xf, false));
}
DEV float sum16(float v) {
  v += dpp<0x128>(v); v += dpp<0x124>(v); v += dpp<0x122>(v); v += dpp<0x121>(v);
  return v;
}
DEV float other_half(float v) {
  return BC(float, __builtin_amdgcn_ds_bpermute(((threadIdx.x & 63) ^ 32) << 2, BC(int, v)));
}

DEV rsrc_t mkrsrc(const char* p, int bytes) { return __builtin_amdgcn_make_buffer_rsrc((void*)p, 0, bytes, 0x00020000); }
template <int AUX> DEV u32x4 bld(rsrc_t r, int vo, int so) { return BC(u32x4, __builtin_amdgcn_raw_buffer_load_b128(r, vo, so, AUX)); }
DEV u32x4 mksrd(const void* p, unsigned bytes) {
  const uint64_t a = (uint64_t)p;
  return (u32x4){(unsigned)a, (unsigned)(a >> 32) & 0xffffu, bytes, 0x00020000u};
}
DEV unsigned lds_addr(const char* p) { return (unsigned)(uintptr_t)(const __attribute__((address_space(3))) char*)p; }
DEV void dma(unsigned dst, int vo, u32x4 srd, int so) {
  asm volatile("s_mov_b32 m0, %0\n\ts_nop 0\n\tbuffer_load_dwordx4 %1, %2, %3 offen lds" ::"s"(dst), "v"(vo), "s"(srd), "s"(so) : "memory");
}
DEV void dma_sc1(unsigned dst, int vo, u32x4 srd, int so) {
  asm volatile("s_mov_b32 m0, %0\n\ts_nop 0\n\tbuffer_load_dwordx4 %1, %2, %3 offen sc1 lds" ::"s"(dst), "v"(vo), "s"(srd), "s"(so) : "memory");
}
DEV void dma_half(unsigned dst, int vo, u32x4 srd, int so) {
  asm volatile("s_mov_b32 m0, %0\n\ts_mov_b32 exec_hi, 0\n\tbuffer_load_dwordx4 %1, %2, %3 offen lds\n\ts_mov_b32 exec_hi, -1"
               ::"s"(dst), "v"(vo), "s"(srd), "s"(so) : "memory");
}
template <int N> DEV void vm_barrier() {
  asm volatile("s_waitcnt vmcnt(%0)\n\ts_barrier" ::"i"(N) : "memory");
}
#define SLP 2
#define SPINMAX 0
DEV void st128(rsrc_t r, int vo, u32x4 v) { __builtin_amdgcn_raw_buffer_store_b128(v, r, vo, 0, 16); }
DEV void st64(rsrc_t r, int vo, u32x2 v) { __builtin_amdgcn_raw_buffer_store_b64(v, r, vo, 0, 16); }
DEV u32x2 next_epoch(char* ws) {
  const u32x2 e = BC(u32x2, __builtin_amdgcn_raw_buffer_load_b64(mkrsrc(ws + OFF_FLAG, FL_BYTES), FL_EPOCH, 0, 16));
  const unsigned e0 = e.x, e1 = e.y;
  const uint64_t v = (((uint64_t)(unsigned)__builtin_amdgcn_readfirstlane(e1) << 32) | (unsigned)__builtin_amdgcn_readfirstlane(e0)) + 1;
  return (u32x2){(unsigned)v, (unsigned)(v >> 32)};
}
DEV void signal(char* ws, int off, u32x2 ep, int replicas = 1) {
  asm volatile("s_waitcnt vmcnt(0)" ::: "memory");
  __syncthreads();
  if (threadIdx.x < replicas) st64(mkrsrc(ws + OFF_FLAG, FL_BYTES), off + threadIdx.x * 1024, ep);
}
DEV bool raised(u32x4 v, u32x2 ep) {
  return __builtin_amdgcn_ballot_w64(v.x != ep.x || v.y != ep.y || v.z != ep.x || v.w != ep.y) == 0;
}
DEV u32x4 flag_ld(u32x4 srd, int vo) {
  u32x4 v;
  asm volatile("buffer_load_dwordx4 %0, %1, %2, 0 offen sc1\n\ts_waitcnt vmcnt(0)" : "=v"(v) : "v"(vo), "s"(srd) : "memory");
  return v;
}
DEV void await(char* ws, int vo, u32x2 ep) {
  const u32x4 srd = mksrd(ws + OFF_FLAG, FL_BYTES);
  for (int it = 0; !raised(flag_ld(srd, vo), ep); it++) {
    if (SPINMAX && it > SPINMAX) break;
    __builtin_amdgcn_s_sleep(SLP);
  }
}

template <class F> DEV void layernorm(char* lds, const u16* __restrict__ x, const u16* __restrict__ wk, const u16* __restrict__ wv,
                                      char* __restrict__ ws, int b, F more) {
  const int t = threadIdx.x, lane = t & 63, w = __builtin_amdgcn_readfirstlane(t >> 6);
  const int tok = t >> 4, c0 = t & 15;
  u32x4 xv[4], kw[4], vw[4];
#pragma unroll
  for (int i = 0; i < 4; i++) xv[i] = *(const u32x4*)(x + (b * 16 + tok) * 512 + (c0 + 16 * i) * 8);
#pragma unroll
  for (int i = 0; i < 4; i++) { kw[i] = ((const u32x4*)wk)[t + 256 * i]; vw[i] = ((const u32x4*)wv)[t + 256 * i]; }
  __builtin_amdgcn_sched_barrier(0);
  more();
  __builtin_amdgcn_sched_barrier(0);
  float f[32];
#pragma unroll
  for (int i = 0; i < 4; i++)
#pragma unroll
    for (int j = 0; j < 4; j++) { f[i * 8 + 2 * j] = lo16(xv[i][j]); f[i * 8 + 2 * j + 1] = hi16(xv[i][j]); }
  float s = 0;
#pragma unroll
  for (int i = 0; i < 32; i++) s += f[i];
  const float mean = sum16(s) * (1.0f / 512);
  float q = 0;
#pragma unroll
  for (int i = 0; i < 32; i++) { f[i] -= mean; q += f[i] * f[i]; }
  const float rstd = 1.0f / sqrtf(sum16(q) * (1.0f / 512) + 1e-5f);
#pragma unroll
  for (int i = 0; i < 4; i++) {
    u32x4 o;
#pragma unroll
    for (int j = 0; j < 4; j++) o[j] = pk(f[i * 8 + 2 * j] * rstd, f[i * 8 + 2 * j + 1] * rstd);
    *(u32x4*)(lds + (c0 + 16 * i) * 256 + ((tok ^ c0) * 16)) = o;
  }
#pragma unroll
  for (int i = 0; i < 4; i++) {
    *(u32x4*)(lds + 16384 + (t + 256 * i) * 16) = kw[i];
    *(u32x4*)(lds + 32768 + (t + 256 * i) * 16) = vw[i];
  }
  __syncthreads();
  const int n = lane & 15, g = lane >> 4;
  f32x4 ak = {0, 0, 0, 0}, av = {0, 0, 0, 0};
  const rsrc_t rs = mkrsrc(ws + OFF_XN, OFF_WO);
  const int xnf = (b >> 1) * 32768 + ((b & 1) * 16 + n) * 16;
#pragma unroll
  for (int i = 0; i < 4; i++) {
    const int S = 4 * w + i, c = 4 * S + g;
    const u32x4 xf = *(const u32x4*)(lds + c * 256 + ((n ^ (c & 15)) * 16));
    const char* pw = lds + 16384 + (32 * S + 8 * g + (n >> 2)) * 32 + (n & 3) * 8;
    const u32x4 kf = tr_frag(pw, pw + 128), vf = tr_frag(pw + 16384, pw + 16384 + 128);
    ak = MFMA16(kf, xf, ak);
    av = MFMA16(xf, vf, av);
    st128(rs, xnf + c * 512, xf);
  }
  f32x4* red = (f32x4*)(lds + 49152);
  red[(w * 2 + 0) * 64 + lane] = ak;
  red[(w * 2 + 1) * 64 + lane] = av;
  __syncthreads();
  if (w < 2) {
    const f32x4 r = red[(0 + w) * 64 + lane] + red[(2 + w) * 64 + lane] + red[(4 + w) * 64 + lane] + red[(6 + w) * 64 + lane];
    const u32x2 o = {pk(r[0], r[1]), pk(r[2], r[3])};
    st64(rs, w == 0 ? (int)OFF_KF + ((b >> 1) * 64 + (g & 1) * 32 + (b & 1) * 16 + n) * 16 + (g >> 1) * 8
                    : (int)OFF_VF + b * 512 + (g >> 1) * 256 + n * 16 + (g & 1) * 8, o);
  }
}
template <int N, class F> DEV void retile(char* lds, const u16* __restrict__ wo, const u16* __restrict__ wd, char* __restrict__ ws, int sl0, F more) {
  const int t = threadIdx.x, lane = t & 63, w = __builtin_amdgcn_readfirstlane(t >> 6);
  u32x4 v[N][4];
#pragma unroll
  for (int e = 0; e < N; e++) {
    const int sl = sl0 + e;
    const u16* src = sl < 32 ? wo + sl * 8192 : wd + (sl - 32) * 8192;
#pragma unroll
    for (int i = 0; i < 4; i++) v[e][i] = ((const u32x4*)src)[t + 256 * i];
  }
  __builtin_amdgcn_sched_barrier(0);
  more();
  __builtin_amdgcn_sched_barrier(0);
#pragma unroll
  for (int e = 0; e < N; e++)
#pragma unroll
    for (int i = 0; i < 4; i++) *(u32x4*)(lds + e * 17408 + (w + 4 * i) * 1088 + lane * 16) = v[e][i];
  __syncthreads();
  const int h = lane >> 5, blk = (lane >> 4) & 1, q = (lane & 15) >> 2, p = lane & 3;
  const int r0 = 4 * h + q, dr = 8;
  const rsrc_t rs = mkrsrc(ws + OFF_WO, 2304 << 10);
#pragma unroll
  for (int e = 0; e < N; e++) {
    const int sl = sl0 + e, ns = sl < 32 ? 32 : 112, base = sl < 32 ? sl : 512 + sl - 32;
#pragma unroll
    for (int i = 0; i < 4; i++) {
      const int j = 4 * w + i;
      const char* pp = lds + e * 17408 + r0 * 1088 + j * 64 + blk * 32 + p * 8;
      st128(rs, (j * ns + base) * 1024 + lane * 16, tr_frag(pp, pp + dr * 1088));
    }
  }
}
template <int DEPTH> DEV void tileA(u32x4 (&r)[DEPTH][4], rsrc_t ra, int a0, int a1, int lo) {
#pragma unroll
  for (int s = 0; s < DEPTH; s++) { r[s][0] = bld<16>(ra, lo, a0 + s * 1024); r[s][1] = bld<16>(ra, lo, a1 + s * 1024); }
}
template <int NS, int DEPTH, int AUX>
DEV void tile64(u32x4 (&r)[DEPTH][4], rsrc_t ra, int a0, int a1, rsrc_t rb, int b0, int b1, int lo, f32x16 (&acc)[2][2]) {
#pragma unroll
  for (int s = 0; s < DEPTH; s++) { r[s][2] = bld<AUX>(rb, lo, b0 + s * 1024); r[s][3] = bld<AUX>(rb, lo, b1 + s * 1024); }
  __builtin_amdgcn_sched_barrier(0);
#pragma unroll
  for (int s = 0; s < NS; s++) {
    const int d = s % DEPTH;
    acc[0][0] = MFMA32(r[d][0], r[d][2], acc[0][0]);
    acc[1][0] = MFMA32(r[d][1], r[d][2], acc[1][0]);
    acc[0][1] = MFMA32(r[d][0], r[d][3], acc[0][1]);
    acc[1][1] = MFMA32(r[d][1], r[d][3], acc[1][1]);
    asm volatile("" : "+v"(acc[0][0]), "+v"(acc[1][0]), "+v"(acc[0][1]), "+v"(acc[1][1]));
    if (s + DEPTH < NS) {
      const int o = (s + DEPTH) * 1024;
      r[d][0] = bld<16>(ra, lo, a0 + o); r[d][2] = bld<AUX>(rb, lo, b0 + o);
      r[d][1] = bld<16>(ra, lo, a1 + o); r[d][3] = bld<AUX>(rb, lo, b1 + o);
    }
    __builtin_amdgcn_sched_barrier(0);
  }
}
DEV void zero4(f32x16 (&acc)[2][2]) {
#pragma unroll
  for (int i = 0; i < 2; i++)
#pragma unroll
    for (int j = 0; j < 2; j++)
#pragma unroll
      for (int r = 0; r < 16; r++) acc[i][j][r] = 0.f;
}
constexpr int RP = 1056;
DEV void red_write(char* lds, int w, int lane, const f32x16 (&acc)[2][2]) {
#pragma unroll
  for (int j = 0; j < 2; j++)
#pragma unroll
    for (int i = 0; i < 2; i++)
#pragma unroll
      for (int q = 0; q < 4; q++)
        *(f32x4*)(lds + (((w * 2 + j) * 2 + i) * 4 + q) * RP + lane * 16) =
            (f32x4){acc[i][j][4 * q], acc[i][j][4 * q + 1], acc[i][j][4 * q + 2], acc[i][j][4 * q + 3]};
}
DEV void red_read8(const char* lds, int j, int cg, int tk, float (&o)[8]) {
  f32x4 a = {0, 0, 0, 0}, b = {0, 0, 0, 0};
#pragma unroll
  for (int w = 0; w < 4; w++) {
    const char* p = lds + ((w * 2 + j) * 8 + cg) * RP + tk * 16;
    a += *(const f32x4*)p; b += *(const f32x4*)(p + 512);
  }
#pragma unroll
  for (int e = 0; e < 4; e++) { o[e] = a[e]; o[4 + e] = b[e]; }
}

constexpr int LDS2 = 78848;

DEV void exp8(f32x8& p, const f32x16& s, int o) {
#pragma unroll
  for (int i = 0; i < 8; i++) p[i] = __builtin_amdgcn_exp2f(s[o + i]);
}
DEV float fmx(float a, float b) { return __builtin_elementwise_maximum(a, b); }
DEV float max16(const f32x16& s) {
  float m = fmx(fmx(s[0], s[1]), s[2]), n = fmx(fmx(s[3], s[4]), s[5]);
#pragma unroll
  for (int i = 6; i < 14; i += 4) { m = fmx(fmx(m, s[i]), s[i + 1]); n = fmx(fmx(n, s[i + 2]), s[i + 3]); }
  return fmx(fmx(fmx(m, s[14]), s[15]), n);
}
DEV f32x16 splat16(float v) { f32x16 c;
#pragma unroll
  for (int i = 0; i < 16; i++) c[i] = v;
  return c;
}

DEV void attention(char* lds, char* __restrict__ ws, int tbp, int hg, int rep, u32x2 ep, const u32x4 (&wraw)[16]) {
  const int t = threadIdx.x, lane = t & 63, w = __builtin_amdgcn_readfirstlane(t >> 6), tb = w >> 1, kh = w & 1;
  const int lo = lane * 16;
  const rsrc_t krs = mkrsrc(ws + OFF_KF, 65536), vrs = mkrsrc(ws + OFF_VF, 65536);
  const int m_ = lane & 31;
  constexpr int VP = 1056, HALF = 4096 + 4 * VP, SLOT = 2 * HALF;
  const u32x4 ksrd = mksrd(ws + OFF_KF, 131072);
  const unsigned ring = lds_addr(lds) + kh * HALF + tb * 4096;
  const int gsrc = tb * 65536 + kh * 32768;
  auto fetch1 = [&](int c, int slot, int i) { dma_sc1(ring + slot * SLOT + i * (tb ? VP : 1024), lo, ksrd, gsrc + (c * 4 + i) * 1024); };
  auto fetch = [&](int c, int slot) {
#pragma unroll
    for (int i = 0; i < 4; i++) fetch1(c, slot, i);
  };
  const int pad = (m_ == 16 || m_ == 20) ? 1024 : 1040;
  const char* kp = lds + kh * HALF + lo;
  const char* vp0 = lds + kh * HALF + 4096 + (m_ < 16 ? (lane >> 5) * 256 + m_ * 16 : pad);
  const char* vp1 = lds + kh * HALF + 4096 + (m_ < 16 ? 512 + (lane >> 5) * 256 + m_ * 16 : pad);
  const u32x4 ones = {0x3f803f80u, 0x3f803f80u, 0x3f803f80u, 0x3f803f80u};
#define LDK(kb) bld<16>(krs, lo, (kb) * 1024)
  u32x4 q0, q1, q2, q3;
  float m0, m1, m2, m3;
  const int kb0 = kh * 32;
  u32x4 kf, va, vb;
  {
    f32x16 acc[2][2]; zero4(acc);
    u32x4 r[8][4];
    const rsrc_t rb = mkrsrc(ws + OFF_XN, 2 << 20);
    const int b0 = ((tbp * 2 + tb) * 32 + 8 * w) * 1024;
    {
      char* img = lds + w * 16384;
#pragma unroll
      for (int i = 0; i < 16; i++) *(u32x4*)(img + i * 1024 + lo) = wraw[i];
      const int h = lane >> 5, blk = (lane >> 4) & 1, q = (lane & 15) >> 2, p = lane & 3;
      const char* pp = img + (8 * h + q) * 128 + blk * 32 + p * 8;
      const int j0 = (kh ^ (q >> 1)) * 64;
#pragma unroll
      for (int s = 0; s < 8; s++) {
        r[s][0] = tr_frag(pp + s * 2048 + j0, pp + s * 2048 + 512 + j0);
        r[s][1] = tr_frag(pp + s * 2048 + (j0 ^ 64), pp + s * 2048 + 512 + (j0 ^ 64));
      }
    }
    if (w == 0) await(ws, FL_LN + rep * 1024 + lo, ep);
    __syncthreads();
#pragma unroll
    for (int s = 0; s < 8; s++) { r[s][2] = bld<16>(rb, lo, b0 + s * 1024); r[s][3] = bld<16>(rb, lo, (b0 ^ 32768) + s * 1024); }
    const u32x4 k0 = LDK(0), k1 = LDK(1);
    __builtin_amdgcn_sched_barrier(0);
    fetch(0, 0);
    __builtin_amdgcn_sched_barrier(0);
#pragma unroll
    for (int s = 0; s < 8; s++) {
      acc[0][0] = MFMA32(r[s][0], r[s][2], acc[0][0]);
      acc[1][0] = MFMA32(r[s][1], r[s][2], acc[1][0]);
      acc[0][1] = MFMA32(r[s][0], r[s][3], acc[0][1]);
      acc[1][1] = MFMA32(r[s][1], r[s][3], acc[1][1]);
      asm volatile("" : "+v"(acc[0][0]), "+v"(acc[1][0]), "+v"(acc[0][1]), "+v"(acc[1][1]));
      __builtin_amdgcn_sched_barrier(0);
    }
#pragma unroll
    for (int x = 1; x < 4; x++) {
      const f32x16& a = acc[x & 1][x >> 1];
      char* p = lds + SLOT + (((w ^ x) * 3 + x - 1) * 4) * 1024 + lo;
#pragma unroll
      for (int q = 0; q < 4; q++) *(f32x4*)(p + q * 1024) = (f32x4){a[4 * q], a[4 * q + 1], a[4 * q + 2], a[4 * q + 3]};
    }
    __syncthreads();
    const float sc = 0.25f * 1.4426950408889634f;
    unsigned qq[8];
#pragma unroll
    for (int q = 0; q < 4; q++) {
      const char* p = lds + SLOT + (w * 12 + q) * 1024 + lo;
      f32x4 a = *(const f32x4*)p + *(const f32x4*)(p + 4096) + *(const f32x4*)(p + 8192);
#pragma unroll
      for (int e = 0; e < 4; e++) a[e] = (a[e] + acc[0][0][4 * q + e]) * sc;
      qq[2 * q] = pk(a[0], a[1]); qq[2 * q + 1] = pk(a[2], a[3]);
    }
    q0 = (u32x4){qq[0], qq[1], qq[2], qq[3]}; q1 = (u32x4){qq[4], qq[5], qq[6], qq[7]};
    const f32x16 z = splat16(0.f);
    const f32x16 t0 = MFMA32(k0, q0, z), t1 = MFMA32(k1, q0, z), t2 = MFMA32(k0, q1, z), t3 = MFMA32(k1, q1, z);
    m0 = fmx(max16(t0), max16(t1));
    m1 = fmx(max16(t2), max16(t3));
    m0 = fmx(m0, other_half(m0)) + 64.f;
    m1 = fmx(m1, other_half(m1)) + 64.f;
    char* xq = lds + 4 * SLOT;
    *(u32x4*)(xq + (w * 3) * 1024 + lo) = q0;
    *(u32x4*)(xq + (w * 3 + 1) * 1024 + lo) = q1;
    *(f32x2*)(xq + (w * 3 + 2) * 1024 + lane * 8) = (f32x2){m0, m1};
    const u32x4 fill = (t & 1) ? (u32x4){0, 0, 0, 0} : ones;
    if (t < 16) *(u32x4*)(lds + (t >> 3) * HALF + 4096 + ((t >> 1) & 3) * VP + 1024 + (t & 1) * 16) = fill;
    asm volatile("s_waitcnt vmcnt(0)" ::: "memory");
    __syncthreads();
    fetch(1, 1); fetch(2, 2);
    if (t < 48) *(u32x4*)(lds + SLOT + (t >> 3) * HALF + 4096 + ((t >> 1) & 3) * VP + 1024 + (t & 1) * 16) = fill;
    kf = *(const u32x4*)kp; va = *(const u32x4*)vp0; vb = va;
    q2 = *(const u32x4*)(xq + ((w ^ 1) * 3) * 1024 + lo);
    q3 = *(const u32x4*)(xq + ((w ^ 1) * 3 + 1) * 1024 + lo);
    const f32x2 mm = *(const f32x2*)(xq + ((w ^ 1) * 3 + 2) * 1024 + lane * 8);
    m2 = mm[0]; m3 = mm[1];
  }
  f32x16 o0, o1, o2, o3;
  {
    const f32x16 c0 = splat16(-m0), c1 = splat16(-m1), c2 = splat16(-m2), c3 = splat16(-m3);
    o0 = o1 = o2 = o3 = splat16(0.f);
    f32x16 s0 = MFMA32(kf, q0, c0), s1 = MFMA32(kf, q1, c1), s2 = MFMA32(kf, q2, c2), s3 = splat16(0.f);
#define EXPN(s, o, n) do { _Pragma("unroll") for (int i_ = 0; i_ < n; i_++) { float e_ = __builtin_amdgcn_exp2f(s[(o) + i_]); PIN(e_); s[(o) + i_] = e_; } } while (0)
#define LO8(s) __builtin_shufflevector(s, s, 0, 1, 2, 3, 4, 5, 6, 7)
#define HI8(s) __builtin_shufflevector(s, s, 8, 9, 10, 11, 12, 13, 14, 15)
#define TILE(X, OX, Y, OY, QY, CY, H0, H1, H2) do { \
      EXPN(X, 0, 3); OY = SMF(vb, HI8(Y), OY); PIN(OY); H0; \
      EXPN(X, 3, 5); Y = MFMA32(kf, QY, CY); PIN(Y); H1; \
      EXPN(X, 8, 5); OX = SMF(va, LO8(X), OX); PIN(OX); H2; \
      EXPN(X, 13, 3); } while (0)
    auto chunk = [&](int c, auto first) {
      const int cur = (c & 3) * SLOT, nxt = ((c + 1) & 3) * SLOT;
#pragma unroll
      for (int i = 0; i < 4; i++) {
        if (i == 3) vm_barrier<first ? 4 : 0>();
        const int o1_ = i < 3 ? cur : nxt, i1 = (i + 1) & 3;
        TILE(s0, o0, s3, o3, q3, c3, vb = *(const u32x4*)(vp1 + cur + i * VP), kf = *(const u32x4*)(kp + o1_ + i1 * 1024),
             if (first && i == 3) fetch1(c + 3, (c + 3) & 3, 0));
        TILE(s1, o1, s0, o0, q0, c0, , , if (first && i == 3) fetch1(c + 3, (c + 3) & 3, 1));
        TILE(s2, o2, s1, o1, q1, c1, , , if (first && i == 3) fetch1(c + 3, (c + 3) & 3, 2));
        TILE(s3, o3, s2, o2, q2, c2, if (first && i == 3) fetch1(c + 3, (c + 3) & 3, 3), , va = *(const u32x4*)(vp0 + o1_ + i1 * VP));
      }
    };
    for (int c = 0; c < 5; c++) chunk(c, std::true_type{});
    for (int c = 5; c < 8; c++) chunk(c, std::false_type{});
    o3 = SMF(vb, HI8(s3), o3);
  }
  float r[2][9];
  bool second = false;
  for (;;) {
#pragma unroll
    for (int e = 0; e < 2; e++) {
      const f32x16 s = e ? o3 : o2;
      char* p = lds + ((w * 2 + e) * 3) * 1024;
      *(f32x4*)(p + lo) = (f32x4){s[0], s[1], s[2], s[3]};
      *(f32x4*)(p + 1024 + lo) = (f32x4){s[4], s[5], s[6], s[7]};
      *(float*)(p + 2048 + lane * 4) = s[8];
    }
    __syncthreads();
    bool bad = false;
#pragma unroll
    for (int e = 0; e < 2; e++) {
      const f32x16 s = e ? o1 : o0;
      const char* p = lds + (((w ^ 1) * 2 + e) * 3) * 1024;
      const f32x4 a = *(const f32x4*)(p + lo), b = *(const f32x4*)(p + 1024 + lo);
      const float l = s[8] + *(const float*)(p + 2048 + lane * 4);
      float am = 0.f;
#pragma unroll
      for (int i = 0; i < 4; i++) {
        r[e][i] = s[i] + a[i]; r[e][4 + i] = s[4 + i] + b[i];
        am += fabsf(r[e][i]) + fabsf(r[e][4 + i]);
      }
      r[e][8] = l;
      bad |= !(l >= 0x1p-100f && l <= 0x1p120f && am <= 0x1p126f);
    }
    if (second) break;
    int* vote = (int*)(lds + 30720);
    if (lane == 0) vote[w] = 0;
    if (bad) vote[w] = 1;
    __syncthreads();
    if (__builtin_amdgcn_readfirstlane(vote[0] | vote[1] | vote[2] | vote[3]) == 0) break;
    second = true;
    float* mx = (float*)(lds + 32768);
    for (int hd = 0; hd < 4; hd++) {
      const u32x4 q = hd == 0 ? q0 : hd == 1 ? q1 : hd == 2 ? q2 : q3;
      const f32x16 z = splat16(0.f);
      float m = -3.0e38f;
      for (int kb = kb0; kb < kb0 + 32; kb++) m = fmaxf(m, max16(MFMA32(LDK(kb), q, z)));
      mx[(hd * 4 + w) * 64 + lane] = fmaxf(m, other_half(m));
    }
    __syncthreads();
    for (int hd = 0; hd < 4; hd++) {
      const u32x4 q = hd == 0 ? q0 : hd == 1 ? q1 : hd == 2 ? q2 : q3;
      const float m = fmaxf(mx[(hd * 4 + w) * 64 + lane], mx[((hd ^ 2) * 4 + (w ^ 1)) * 64 + lane]);
      const f32x16 c = splat16(-m);
      f32x16 oo = splat16(0.f);
      for (int kb = kb0; kb < kb0 + 32; kb++) {
        const f32x16 s = MFMA32(LDK(kb), q, c);
        f32x8 p0, p1;
        const int vo = (lane >> 5) * 256 + (m_ & 15) * 16;
        const u32x4 v0 = bld<16>(vrs, vo, kb * 1024), v1 = bld<16>(vrs, vo, kb * 1024 + 512);
        exp8(p0, s, 0); oo = SMF(m_ < 16 ? v0 : ones, p0, oo);
        exp8(p1, s, 8); oo = SMF(m_ < 16 ? v1 : ones, p1, oo);
      }
      if (hd == 0) o0 = oo; else if (hd == 1) o1 = oo; else if (hd == 2) o2 = oo; else o3 = oo;
    }
  }
  const rsrc_t ors = mkrsrc(ws + OFF_O, 2 << 20);
#pragma unroll
  for (int e = 0; e < 2; e++) {
    const float il = 1.0f / r[e][8];
    const u32x4 v = {pk(r[e][0] * il, r[e][1] * il), pk(r[e][2] * il, r[e][3] * il), pk(r[e][4] * il, r[e][5] * il),
                     pk(r[e][6] * il, r[e][7] * il)};
    __builtin_amdgcn_raw_buffer_store_b128(v, ors, lo + ((tbp * 2 + tb) * 32 + hg * 4 + 2 * kh + e) * 1024, 0, 16);
  }
  signal(ws, FL_O + (tbp * 4 + (hg >> 1)) * 128 + (hg & 1) * 8, ep);
#undef LDK
}

constexpr int UPOFF = 32 * 224 + 128, STAGE = 2 * 32 * 224 + 128;
DEV void gemm1(char* lds, const u16* __restrict__ wg, const u16* __restrict__ wu, char* __restrict__ ws, int mt, int nt, int pfx, u32x2& ep) {
  const int t = threadIdx.x, lane = t & 63, w = __builtin_amdgcn_readfirstlane(t >> 6);
  const u32x4 wsrd = mksrd((w & 2) ? wu : wg, 512 * 3584), xsrd = mksrd(ws + OFF_XN, 2 << 20);
  int go[4];
#pragma unroll
  for (int i = 0; i < 4; i++) { const int id = i * 64 + lane, row = id / 14; go[i] = row * 3584 + (id - row * 14) * 16; }
  const int wsrc = nt * 224 + (w & 1) * 16 * 3584, lo = lane * 16, tb = mt * 4 + w;
  const unsigned ldst = lds_addr(lds) + (w >> 1) * UPOFF + (w & 1) * 3584;
  const int h = lane >> 5, blk = (lane >> 4) & 1, q = (lane & 15) >> 2, p = lane & 3;
  const char* pa = lds + blk * UPOFF + (8 * h + q) * 224 + p * 8;
  f32x16 acc[7];
#pragma unroll
  for (int i = 0; i < 7; i++) acc[i] = splat16(0.f);
  u32x4 bb[5][2], f[7];
  auto issue1 = [&](int c, int i) {
    const int st = c % 5;
    if (i < 3) dma(ldst + st * STAGE + i * 1024, go[i], wsrd, wsrc + c * (32 * 3584));
    else if (i == 3) dma_half(ldst + st * STAGE + 3072, go[3], wsrd, wsrc + c * (32 * 3584));
    else if (i < 6)
      asm volatile("buffer_load_dwordx4 %0, %1, %2, %3 offen sc1" : "=v"(bb[st][i - 4]) : "v"(lo), "s"(xsrd), "s"(tb * 32768 + (c * 2 + i - 4) * 1024) : "memory");
  };
  auto issue = [&](int c) {
#pragma unroll
    for (int i = 0; i < 6; i++) issue1(c, i);
  };
  auto rd = [&](int c, int s, int j) {
    const char* pp = pa + (c % 5) * STAGE + s * 3584 + j * 32;
    return tr_frag(pp, pp + 896);
  };
#pragma unroll
  for (int c = 0; c < 4; c++)
#pragma unroll
    for (int i = 0; i < 4; i++) issue1(c, i);
  ep = next_epoch(ws);
  await(ws, FL_LN + pfx * 1024 + tb * 16, ep);
#pragma unroll
  for (int c = 0; c < 4; c++) { issue1(c, 4); issue1(c, 5); }
  const u32x4 fsrd = mksrd(ws + OFF_FLAG, FL_BYTES);
  const int rto = FL_RT + min(lane, 35) * 16;
  u32x4 rtf;
  vm_barrier<6>();
  asm volatile("" : "+v"(bb[0][0]), "+v"(bb[0][1]));
#pragma unroll
  for (int j = 0; j < 7; j++) f[j] = rd(0, 0, j);
#pragma unroll
  for (int c = 0; c < 16; c++) {
#pragma unroll
    for (int j = 0; j < 7; j++) {
      acc[j] = MFMA32(f[j], bb[c % 5][0], acc[j]); PIN(acc[j]);
      f[j] = rd(c, 1, j);
    }
    if (c < 15) {
      if (c == 0) vm_barrier<4>(); else if (c == 1) vm_barrier<8>(); else if (c < 13) vm_barrier<12>(); else if (c == 13) vm_barrier<6>(); else vm_barrier<0>();
      asm volatile("" : "+v"(bb[(c + 1) % 5][0]), "+v"(bb[(c + 1) % 5][1]));
      if (!SPREAD && c < 12) issue(c + 4);
      __builtin_amdgcn_sched_barrier(0);
    }
#pragma unroll
    for (int j = 0; j < 7; j++) {
      acc[j] = MFMA32(f[j], bb[c % 5][1], acc[j]); PIN(acc[j]);
      if (c < 15) f[j] = rd(c + 1, 0, j);
      if (SPREAD && c < 12) { issue1(c + 4, j); __builtin_amdgcn_sched_barrier(0); }
    }
    if (c == 12) asm volatile("buffer_load_dwordx4 %0, %1, %2, 0 offen sc1" : "=v"(rtf) : "v"(rto), "s"(fsrd) : "memory");
    if ((c & 3) == 3) { }
  }
  __builtin_amdgcn_s_setprio(2);
  const rsrc_t hrs = mkrsrc(ws + OFF_H, 7168 << 10);
  auto put = [&](const f32x16& a, int tb, int fb) {
    float hv[8];
#pragma unroll
    for (int r = 0; r < 8; r++) { const float gt = a[r]; hv[r] = BC(float, max(BC(int, gt), 0)) * a[8 + r]; }
    __builtin_amdgcn_raw_buffer_store_b128((u32x4){pk(hv[0], hv[1]), pk(hv[2], hv[3]), pk(hv[4], hv[5]), pk(hv[6], hv[7])}, hrs,
                                           lane * 16 + (tb * 112 + fb) * 1024, 0, 16);
  };
#pragma unroll
  for (int j = 0; j < 7; j++) put(acc[j], tb, nt * 7 + j);
  asm volatile("" : "+v"(rtf));
  if (!raised(rtf, ep)) await(ws, rto, ep);
}

DEV void tail(char* lds, const u16* __restrict__ x, char* __restrict__ ws, u16* __restrict__ out, int hidx, int tbp, int ct, u32x2 ep) {
  const int t = threadIdx.x, lane = t & 63, w = __builtin_amdgcn_readfirstlane(t >> 6), lo = lane * 16;
  const int cg = t & 7, tk = t >> 3;
  f32x16 acc[2][2];
  u32x4 r[8][4];
  float g[2][8];
  {
    const rsrc_t ra = mkrsrc(ws + OFF_WD, 1792 << 10), rb = mkrsrc(ws + OFF_H, 7168 << 10);
    const int a0 = (ct * 2 * 112 + 28 * w) * 1024, b0 = (tbp * 2 * 112 + 28 * w) * 1024;
    signal(ws, FL_H + (hidx >> 2) * 128 + (hidx & 3) * 8, ep);
    tileA<8>(r, ra, a0, a0 + 112 * 1024, lo);
    zero4(acc);
    await(ws, FL_H + ((tbp >> 1) * 4 + w) * 128 + (lane & 1) * 16, ep);
    __builtin_amdgcn_s_setprio(GPRIO);
    tile64<28, 8, 16>(r, ra, a0, a0 + 112 * 1024, rb, b0, b0 + 112 * 1024, lo, acc);
    __builtin_amdgcn_s_setprio(2);
  }
  const rsrc_t ra = mkrsrc(ws + OFF_WO, 512 << 10), rb = mkrsrc(ws + OFF_O, 2 << 20);
  const int a0 = (ct * 2 * 32 + 8 * w) * 1024, b0 = (tbp * 2 * 32 + 8 * w) * 1024;
  tileA<8>(r, ra, a0, a0 + 32768, lo);
  u32x4 xv[2];
#pragma unroll
  for (int j = 0; j < 2; j++) xv[j] = *(const u32x4*)(x + (tbp * 64 + j * 32 + tk) * 512 + ct * 64 + cg * 8);
  red_write(lds, w, lane, acc);
  __syncthreads();
#pragma unroll
  for (int j = 0; j < 2; j++) {
    red_read8(lds, j, cg, tk, g[j]);
#pragma unroll
    for (int e = 0; e < 8; e++) g[j][e] = g[j][e] / (1.0f + __expf(-g[j][e]));
  }
  zero4(acc);
  __syncthreads();
  await(ws, FL_O + (tbp * 4 + w) * 128, ep);
  tile64<8, 8, 16>(r, ra, a0, a0 + 32768, rb, b0, b0 + 32768, lo, acc);
  red_write(lds, w, lane, acc);
  __syncthreads();
#pragma unroll
  for (int j = 0; j < 2; j++) {
    float s[8], o[8];
    red_read8(lds, j, cg, tk, s);
#pragma unroll
    for (int e = 0; e < 8; e++) o[e] = ((e & 1) ? hi16(xv[j][e >> 1]) : lo16(xv[j][e >> 1])) + s[e] * g[j][e];
    *(u32x4*)(out + (tbp * 64 + j * 32 + tk) * 512 + ct * 64 + cg * 8) =
        (u32x4){pk(o[0], o[1]), pk(o[2], o[3]), pk(o[4], o[5]), pk(o[6], o[7])};
  }
}

__global__ __launch_bounds__(256, 2) void k2(const u16* __restrict__ x, const u16* __restrict__ wq, const u16* __restrict__ wk,
                                             const u16* __restrict__ wv, const u16* __restrict__ wo, const u16* __restrict__ wg,
                                             const u16* __restrict__ wu, const u16* __restrict__ wd, char* __restrict__ ws,
                                             u16* __restrict__ out) {
  __shared__ __attribute__((aligned(16))) char lds[LDS2];
  const int b = blockIdx.x, xc = b & 7, r = (b >> 3) & 31, lane = threadIdx.x & 63;
  if (b < 256) {
    __builtin_amdgcn_s_setprio(APRIO);
    const u32x2 ep0 = BC(u32x2, __builtin_amdgcn_raw_buffer_load_b64(mkrsrc(ws + OFF_FLAG, FL_BYTES), FL_EPOCH, 0, 16));
    const int tbp = (xc & 1) * 16 + (r & 15), hg = (xc >> 1) * 2 + (r >> 4);
    u32x4 wraw[16];
    const rsrc_t qrs = mkrsrc((const char*)wq, 512 * 1024);
    const int qo = ((threadIdx.x >> 6) * 128 + (lane >> 3)) * 1024 + hg * 128 + (((lane & 7) ^ (((lane >> 4) & 1) * 4)) * 16);
    auto ldq = [&]() {
#pragma unroll
      for (int i = 0; i < 16; i++) wraw[i] = bld<0>(qrs, qo, i * 8192);
    };
    if (b < 128) layernorm(lds, x, wk, wv, ws, b, ldq);
    else if (b < 200) retile<2>(lds, wo, wd, ws, 2 * b - 256, ldq);
    else ldq();
    const unsigned e0 = ep0.x, e1 = ep0.y;
    const uint64_t e = (((uint64_t)(unsigned)__builtin_amdgcn_readfirstlane(e1) << 32) | (unsigned)__builtin_amdgcn_readfirstlane(e0)) + 1;
    const u32x2 ep = {(unsigned)e, (unsigned)(e >> 32)};
    if (b < 128) signal(ws, FL_LN + b * 8, ep, 8);
    else if (b < 200) signal(ws, FL_RT + (b - 128) * 8, ep);
    attention(lds, ws, tbp, hg, xc, ep, wraw);
  } else {
    const int mt = (xc & 1) * 8 + (r & 7), nt = (xc >> 1) * 4 + (r >> 3);
    __builtin_amdgcn_s_setprio(GPRIO);
    u32x2 ep;
    gemm1(lds, wg, wu, ws, mt, nt, xc, ep);
    tail(lds, x, ws, out, mt * 16 + nt, xc * 4 + (r & 3), r >> 2, ep);
    if (b == 511 && threadIdx.x < 64) {
#pragma unroll 1
      for (int i = 0; i < 2; i++) {
        await(ws, FL_H + lane * 128 + i * 16, ep);
        await(ws, FL_O + (i * 64 + lane) * 128, ep);
      }
      if (lane == 0) st64(mkrsrc(ws + OFF_FLAG, FL_BYTES), FL_EPOCH, ep);
    }
  }
}

extern "C" void kernel_launch(void* const* d_in, const int* in_sizes, int n_in, void* d_out, int out_size, void* d_ws,
                              size_t ws_size, hipStream_t stream) {
  k2<<<512, 256, 0, stream>>>((const u16*)d_in[0], (const u16*)d_in[1], (const u16*)d_in[2], (const u16*)d_in[3],
                              (const u16*)d_in[4], (const u16*)d_in[6], (const u16*)d_in[7], (const u16*)d_in[8], (char*)d_ws,
                              (u16*)d_out);
}
```

```cpp
#include <hip/hip_runtime.h>
#include <stdint.h>
#include <type_traits>

typedef unsigned short u16;
typedef __attribute__((ext_vector_type(2))) __bf16 bf16x2;
typedef __attribute__((ext_vector_type(8))) __bf16 bf16x8;
typedef __attribute__((ext_vector_type(16))) __bf16 bf16x16;
typedef __attribute__((ext_vector_type(2))) float f32x2;
typedef __attribute__((ext_vector_type(4))) float f32x4;
typedef __attribute__((ext_vector_type(8))) float f32x8;
typedef __attribute__((ext_vector_type(16))) float f32x16;
typedef __attribute__((ext_vector_type(2))) unsigned u32x2;
typedef __attribute__((ext_vector_type(4))) unsigned u32x4;
typedef __attribute__((ext_vector_type(4))) short s16x4;
typedef __attribute__((address_space(3))) s16x4 lds_s16x4;
typedef __attribute__((address_space(1))) unsigned gu32;
typedef __attribute__((address_space(3))) unsigned lu32;
typedef __amdgpu_buffer_rsrc_t rsrc_t;
#define WARM 1
#define APRIO 2
#define GPRIO 0
#define SPREAD 1
#define PIN(x) do { asm volatile("" : "+v"(x)); __builtin_amdgcn_sched_barrier(0); } while (0)
#define DEV static __device__ __forceinline__
#define BC(T, x) __builtin_bit_cast(T, x)
#define MFMA32(a, b, c) __builtin_amdgcn_mfma_f32_32x32x16_bf16(BC(bf16x8, a), BC(bf16x8, b), c, 0, 0, 0)
#define MFMA16(a, b, c) __builtin_amdgcn_mfma_f32_16x16x32_bf16(BC(bf16x8, a), BC(bf16x8, b), c, 0, 0, 0)
#define SMF(a, b, c) __builtin_amdgcn_smfmac_f32_32x32x32_bf16(BC(bf16x8, a), BC(bf16x16, b), c, 0xDDDD, 0, 0)

constexpr size_t OFF_XN = 0;
constexpr size_t OFF_KF = 2u << 20;
constexpr size_t OFF_VF = OFF_KF + (64u << 10);
constexpr size_t OFF_WO = OFF_VF + (64u << 10);
constexpr size_t OFF_WD = OFF_WO + (512u << 10);
constexpr size_t OFF_H = OFF_WD + (1792u << 10);
constexpr size_t OFF_O = OFF_H + (7168u << 10);
constexpr size_t OFF_FLAG = OFF_O + (2048u << 10);
constexpr int FL_LN = 0, FL_RT = 8192, FL_H = 9216, FL_O = FL_H + 8192, FL_EPOCH = FL_O + 16384, FL_BYTES = FL_EPOCH + 128;

DEV unsigned pk(float a, float b) { return BC(unsigned, __builtin_convertvector((f32x2){a, b}, bf16x2)); }
DEV float lo16(unsigned u) { return BC(float, u << 16); }
DEV float hi16(unsigned u) { return BC(float, u & 0xffff0000u); }
DEV u32x2 tr_read(const char* p) { return BC(u32x2, __builtin_amdgcn_ds_read_tr16_b64_v4i16((lds_s16x4*)p)); }
DEV u32x4 tr_frag(const char* p0, const char* p1) {
  u32x2 a = tr_read(p0), b = tr_read(p1);
  return (u32x4){a.x, a.y, b.x, b.y};
}
template <int C> DEV float dpp(float v) {
  return BC(float, __builtin_amdgcn_update_dpp(0, BC(int, v), C, 0xf, 0xf, false));
}
DEV float sum16(float v) {
  v += dpp<0x128>(v); v += dpp<0x124>(v); v += dpp<0x122>(v); v += dpp<0x121>(v);
  return v;
}
DEV float fmx(float a, float b) { return __builtin_elementwise_maximum(a, b); }
DEV float max_halves(float v) {
  const auto r = __builtin_amdgcn_permlane32_swap(BC(unsigned, v), BC(unsigned, v), false, false);
  const unsigned a = r[0], b = r[1];
  return fmx(BC(float, a), BC(float, b));
}

DEV rsrc_t mkrsrc(const char* p, int bytes) { return __builtin_amdgcn_make_buffer_rsrc((void*)p, 0, bytes, 0x00020000); }
template <int AUX> DEV u32x4 bld(rsrc_t r, int vo, int so) { return BC(u32x4, __builtin_amdgcn_raw_buffer_load_b128(r, vo, so, AUX)); }
DEV u32x4 mksrd(const void* p, unsigned bytes) {
  const uint64_t a = (uint64_t)p;
  return (u32x4){(unsigned)a, (unsigned)(a >> 32) & 0xffffu, bytes, 0x00020000u};
}
DEV unsigned lds_addr(const char* p) { return (unsigned)(uintptr_t)(const __attribute__((address_space(3))) char*)p; }
DEV void dma(unsigned dst, int vo, u32x4 srd, int so) {
  asm volatile("s_mov_b32 m0, %0\n\ts_nop 0\n\tbuffer_load_dwordx4 %1, %2, %3 offen lds" ::"s"(dst), "v"(vo), "s"(srd), "s"(so) : "memory");
}
DEV void dma_sc1(unsigned dst, int vo, u32x4 srd, int so) {
  asm volatile("s_mov_b32 m0, %0\n\ts_nop 0\n\tbuffer_load_dwordx4 %1, %2, %3 offen sc1 lds" ::"s"(dst), "v"(vo), "s"(srd), "s"(so) : "memory");
}
DEV void dma_half(unsigned dst, int vo, u32x4 srd, int so) {
  asm volatile("s_mov_b32 m0, %0\n\ts_mov_b32 exec_hi, 0\n\tbuffer_load_dwordx4 %1, %2, %3 offen lds\n\ts_mov_b32 exec_hi, -1"
               ::"s"(dst), "v"(vo), "s"(srd), "s"(so) : "memory");
}
template <int N> DEV void vm_barrier() {
  asm volatile("s_waitcnt vmcnt(%0)\n\ts_barrier" ::"i"(N) : "memory");
}
#define LATEQ 1
#define DLY 16
#define DLYG DLY
#define GHOLD 0
#define OAUX 0
#define P_PUT 3
#define P_MID 0
#define P_SILU 0
#define P_FIN 3
#define SLP 2
#define SPINMAX 0
DEV void st128(rsrc_t r, int vo, u32x4 v) { __builtin_amdgcn_raw_buffer_store_b128(v, r, vo, 0, 16); }
DEV void st64(rsrc_t r, int vo, u32x2 v) { __builtin_amdgcn_raw_buffer_store_b64(v, r, vo, 0, 16); }
DEV u32x2 next_epoch(char* ws) {
  const u32x2 e = BC(u32x2, __builtin_amdgcn_raw_buffer_load_b64(mkrsrc(ws + OFF_FLAG, FL_BYTES), FL_EPOCH, 0, 16));
  const unsigned e0 = e.x, e1 = e.y;
  const uint64_t v = (((uint64_t)(unsigned)__builtin_amdgcn_readfirstlane(e1) << 32) | (unsigned)__builtin_amdgcn_readfirstlane(e0)) + 1;
  return (u32x2){(unsigned)v, (unsigned)(v >> 32)};
}
DEV void signal(char* ws, int off, u32x2 ep, int replicas = 1) {
  asm volatile("s_waitcnt vmcnt(0)" ::: "memory");
  __syncthreads();
  if (threadIdx.x < replicas) st64(mkrsrc(ws + OFF_FLAG, FL_BYTES), off + threadIdx.x * 1024, ep);
}
DEV bool raised(u32x4 v, u32x2 ep) {
  return __builtin_amdgcn_ballot_w64(v.x != ep.x || v.y != ep.y || v.z != ep.x || v.w != ep.y) == 0;
}
DEV u32x4 flag_ld(u32x4 srd, int vo) {
  u32x4 v;
  asm volatile("buffer_load_dwordx4 %0, %1, %2, 0 offen sc1\n\ts_waitcnt vmcnt(0)" : "=v"(v) : "v"(vo), "s"(srd) : "memory");
  return v;
}
DEV void await(char* ws, int vo, u32x2 ep) {
  const u32x4 srd = mksrd(ws + OFF_FLAG, FL_BYTES);
  for (int it = 0; !raised(flag_ld(srd, vo), ep); it++) {
    if (SPINMAX && it > SPINMAX) break;
    __builtin_amdgcn_s_sleep(SLP);
  }
}

template <class F> DEV void layernorm(char* lds, const u16* __restrict__ x, const u16* __restrict__ wk, const u16* __restrict__ wv,
                                      char* __restrict__ ws, int b, F more) {
  const int t = threadIdx.x, lane = t & 63, w = __builtin_amdgcn_readfirstlane(t >> 6);
  const int tok = t >> 4, c0 = t & 15;
  u32x4 xv[4], kw[4], vw[4];
#pragma unroll
  for (int i = 0; i < 4; i++) xv[i] = *(const u32x4*)(x + (b * 16 + tok) * 512 + (c0 + 16 * i) * 8);
#pragma unroll
  for (int i = 0; i < 4; i++) { kw[i] = ((const u32x4*)wk)[t + 256 * i]; vw[i] = ((const u32x4*)wv)[t + 256 * i]; }
  __builtin_amdgcn_sched_barrier(0);
  more();
  __builtin_amdgcn_sched_barrier(0);
  float f[32];
#pragma unroll
  for (int i = 0; i < 4; i++)
#pragma unroll
    for (int j = 0; j < 4; j++) { f[i * 8 + 2 * j] = lo16(xv[i][j]); f[i * 8 + 2 * j + 1] = hi16(xv[i][j]); }
  float s = 0;
#pragma unroll
  for (int i = 0; i < 32; i++) s += f[i];
  const float mean = sum16(s) * (1.0f / 512);
  float q = 0;
#pragma unroll
  for (int i = 0; i < 32; i++) { f[i] -= mean; q += f[i] * f[i]; }
  const float rstd = 1.0f / sqrtf(sum16(q) * (1.0f / 512) + 1e-5f);
#pragma unroll
  for (int i = 0; i < 4; i++) {
    u32x4 o;
#pragma unroll
    for (int j = 0; j < 4; j++) o[j] = pk(f[i * 8 + 2 * j] * rstd, f[i * 8 + 2 * j + 1] * rstd);
    *(u32x4*)(lds + (c0 + 16 * i) * 256 + ((tok ^ c0) * 16)) = o;
  }
#pragma unroll
  for (int i = 0; i < 4; i++) {
    *(u32x4*)(lds + 16384 + (t + 256 * i) * 16) = kw[i];
    *(u32x4*)(lds + 32768 + (t + 256 * i) * 16) = vw[i];
  }
  __syncthreads();
  const int n = lane & 15, g = lane >> 4;
  f32x4 ak = {0, 0, 0, 0}, av = {0, 0, 0, 0};
  const rsrc_t rs = mkrsrc(ws + OFF_XN, OFF_WO);
  const int xnf = (b >> 1) * 32768 + ((b & 1) * 16 + n) * 16;
#pragma unroll
  for (int i = 0; i < 4; i++) {
    const int S = 4 * w + i, c = 4 * S + g;
    const u32x4 xf = *(const u32x4*)(lds + c * 256 + ((n ^ (c & 15)) * 16));
    const char* pw = lds + 16384 + (32 * S + 8 * g + (n >> 2)) * 32 + (n & 3) * 8;
    const u32x4 kf = tr_frag(pw, pw + 128), vf = tr_frag(pw + 16384, pw + 16384 + 128);
    ak = MFMA16(kf, xf, ak);
    av = MFMA16(xf, vf, av);
    st128(rs, xnf + c * 512, xf);
  }
  f32x4* red = (f32x4*)(lds + 49152);
  red[(w * 2 + 0) * 64 + lane] = ak;
  red[(w * 2 + 1) * 64 + lane] = av;
  __syncthreads();
  if (w < 2) {
    const f32x4 r = red[(0 + w) * 64 + lane] + red[(2 + w) * 64 + lane] + red[(4 + w) * 64 + lane] + red[(6 + w) * 64 + lane];
    const u32x2 o = {pk(r[0], r[1]), pk(r[2], r[3])};
    st64(rs, w == 0 ? (int)OFF_KF + ((b >> 1) * 64 + (g & 1) * 32 + (b & 1) * 16 + n) * 16 + (g >> 1) * 8
                    : (int)OFF_VF + b * 512 + (g >> 1) * 256 + n * 16 + (g & 1) * 8, o);
  }
}
template <int N, class F> DEV void retile(char* lds, const u16* __restrict__ wo, const u16* __restrict__ wd, char* __restrict__ ws, int sl0, F more) {
  const int t = threadIdx.x, lane = t & 63, w = __builtin_amdgcn_readfirstlane(t >> 6);
  u32x4 v[N][4];
#pragma unroll
  for (int e = 0; e < N; e++) {
    const int sl = sl0 + e;
    const u16* src = sl < 32 ? wo + sl * 8192 : wd + (sl - 32) * 8192;
#pragma unroll
    for (int i = 0; i < 4; i++) v[e][i] = ((const u32x4*)src)[t + 256 * i];
  }
  __builtin_amdgcn_sched_barrier(0);
  more();
  __builtin_amdgcn_sched_barrier(0);
#pragma unroll
  for (int e = 0; e < N; e++)
#pragma unroll
    for (int i = 0; i < 4; i++) *(u32x4*)(lds + e * 17408 + (w + 4 * i) * 1088 + lane * 16) = v[e][i];
  __syncthreads();
  const int h = lane >> 5, blk = (lane >> 4) & 1, q = (lane & 15) >> 2, p = lane & 3;
  const int r0 = 4 * h + q, dr = 8;
  const rsrc_t rs = mkrsrc(ws + OFF_WO, 2304 << 10);
#pragma unroll
  for (int e = 0; e < N; e++) {
    const int sl = sl0 + e, ns = sl < 32 ? 32 : 112, base = sl < 32 ? sl : 512 + sl - 32;
#pragma unroll
    for (int i = 0; i < 4; i++) {
      const int j = 4 * w + i;
      const char* pp = lds + e * 17408 + r0 * 1088 + j * 64 + blk * 32 + p * 8;
      st128(rs, (j * ns + base) * 1024 + lane * 16, tr_frag(pp, pp + dr * 1088));
    }
  }
}
template <int DEPTH> DEV void tileA(u32x4 (&r)[DEPTH][4], rsrc_t ra, int a0, int a1, int lo) {
#pragma unroll
  for (int s = 0; s < DEPTH; s++) { r[s][0] = bld<16>(ra, lo, a0 + s * 1024); r[s][1] = bld<16>(ra, lo, a1 + s * 1024); }
}
template <int NS, int DEPTH, int AUX>
DEV void tile64(u32x4 (&r)[DEPTH][4], rsrc_t ra, int a0, int a1, rsrc_t rb, int b0, int b1, int lo, f32x16 (&acc)[2][2]) {
#pragma unroll
  for (int s = 0; s < DEPTH; s++) { r[s][2] = bld<AUX>(rb, lo, b0 + s * 1024); r[s][3] = bld<AUX>(rb, lo, b1 + s * 1024); }
  __builtin_amdgcn_sched_barrier(0);
#pragma unroll
  for (int s = 0; s < NS; s++) {
    const int d = s % DEPTH;
    acc[0][0] = MFMA32(r[d][0], r[d][2], acc[0][0]);
    acc[1][0] = MFMA32(r[d][1], r[d][2], acc[1][0]);
    acc[0][1] = MFMA32(r[d][0], r[d][3], acc[0][1]);
    acc[1][1] = MFMA32(r[d][1], r[d][3], acc[1][1]);
    asm volatile("" : "+v"(acc[0][0]), "+v"(acc[1][0]), "+v"(acc[0][1]), "+v"(acc[1][1]));
    if (s + DEPTH < NS) {
      const int o = (s + DEPTH) * 1024;
      r[d][0] = bld<16>(ra, lo, a0 + o); r[d][2] = bld<AUX>(rb, lo, b0 + o);
      r[d][1] = bld<16>(ra, lo, a1 + o); r[d][3] = bld<AUX>(rb, lo, b1 + o);
    }
    __builtin_amdgcn_sched_barrier(0);
  }
}
DEV void zero4(f32x16 (&acc)[2][2]) {
#pragma unroll
  for (int i = 0; i < 2; i++)
#pragma unroll
    for (int j = 0; j < 2; j++)
#pragma unroll
      for (int r = 0; r < 16; r++) acc[i][j][r] = 0.f;
}
constexpr int RP = 1056;
DEV void red_write(char* lds, int w, int lane, const f32x16 (&acc)[2][2]) {
#pragma unroll
  for (int j = 0; j < 2; j++)
#pragma unroll
    for (int i = 0; i < 2; i++)
#pragma unroll
      for (int q = 0; q < 4; q++)
        *(f32x4*)(lds + (((w * 2 + j) * 2 + i) * 4 + q) * RP + lane * 16) =
            (f32x4){acc[i][j][4 * q], acc[i][j][4 * q + 1], acc[i][j][4 * q + 2], acc[i][j][4 * q + 3]};
}
DEV void red_read8(const char* lds, int j, int cg, int tk, float (&o)[8]) {
  f32x4 a = {0, 0, 0, 0}, b = {0, 0, 0, 0};
#pragma unroll
  for (int w = 0; w < 4; w++) {
    const char* p = lds + ((w * 2 + j) * 8 + cg) * RP + tk * 16;
    a += *(const f32x4*)p; b += *(const f32x4*)(p + 512);
  }
#pragma unroll
  for (int e = 0; e < 4; e++) { o[e] = a[e]; o[4 + e] = b[e]; }
}

constexpr int LDS2 = 78848;

DEV void exp8(f32x8& p, const f32x16& s, int o) {
#pragma unroll
  for (int i = 0; i < 8; i++) p[i] = __builtin_amdgcn_exp2f(s[o + i]);
}
DEV float max16(const f32x16& s) {
  float m = fmx(fmx(s[0], s[1]), s[2]), n = fmx(fmx(s[3], s[4]), s[5]);
#pragma unroll
  for (int i = 6; i < 14; i += 4) { m = fmx(fmx(m, s[i]), s[i + 1]); n = fmx(fmx(n, s[i + 2]), s[i + 3]); }
  return fmx(fmx(fmx(m, s[14]), s[15]), n);
}
typedef __attribute__((ext_vector_type(8))) double f64x8;
DEV f32x16 splat16w(float v) {
  const f32x2 vv = {v, v};
  const double d = BC(double, vv);
  f64x8 c;
#pragma unroll
  for (int i = 0; i < 7; i++) { double e; asm volatile("v_mov_b64 %0, %1" : "=v"(e) : "v"(d)); c[i] = e; }
  double e; asm volatile("v_mov_b64 %0, %1\n\ts_nop 1" : "=v"(e) : "v"(d)); c[7] = e;
  return BC(f32x16, c);
}
DEV f32x16 zero16w() {
  f64x8 c;
#pragma unroll
  for (int i = 0; i < 8; i++) { double e; asm volatile("v_mov_b64 %0, 0" : "=v"(e)); c[i] = e; }
  return BC(f32x16, c);
}
DEV f32x16 splat16(float v) { f32x16 c;
#pragma unroll
  for (int i = 0; i < 16; i++) c[i] = v;
  return c;
}

DEV void attention(char* lds, char* __restrict__ ws, int tbp, int hg, int rep, u32x2 ep, const u32x4 (&wraw)[16]) {
  const int t = threadIdx.x, lane = t & 63, w = __builtin_amdgcn_readfirstlane(t >> 6), tb = w >> 1, kh = w & 1;
  const int lo = lane * 16;
  const rsrc_t krs = mkrsrc(ws + OFF_KF, 65536), vrs = mkrsrc(ws + OFF_VF, 65536);
  const int m_ = lane & 31;
  constexpr int VP = 1056, HALF = 4096 + 4 * VP, SLOT = 2 * HALF;
  const u32x4 ksrd = mksrd(ws + OFF_KF, 131072);
  const unsigned ring = lds_addr(lds) + kh * HALF + tb * 4096;
  const int gsrc = tb * 65536 + kh * 32768;
  auto fetch1 = [&](int c, int slot, int i) { dma_sc1(ring + slot * SLOT + i * (tb ? VP : 1024), lo, ksrd, gsrc + (c * 4 + i) * 1024); };
  auto fetch = [&](int c, int slot) {
#pragma unroll
    for (int i = 0; i < 4; i++) fetch1(c, slot, i);
  };
  const int pad = (m_ == 16 || m_ == 20) ? 1024 : 1040;
  const char* kp = lds + kh * HALF + lo;
  const char* vp0 = lds + kh * HALF + 4096 + (m_ < 16 ? (lane >> 5) * 256 + m_ * 16 : pad);
  const char* vp1 = lds + kh * HALF + 4096 + (m_ < 16 ? 512 + (lane >> 5) * 256 + m_ * 16 : pad);
  const u32x4 ones = {0x3f803f80u, 0x3f803f80u, 0x3f803f80u, 0x3f803f80u};
#define LDK(kb) bld<16>(krs, lo, (kb) * 1024)
  u32x4 q0, q1, q2, q3;
  float m0, m1, m2, m3;
  const int kb0 = kh * 32;
  u32x4 kf, va, vb;
  f32x16 o0, o1, o2, o3, s3, c0, c1;
  {
    f32x16 acc[2][2]; zero4(acc);
    u32x4 r[8][4];
    const rsrc_t rb = mkrsrc(ws + OFF_XN, 2 << 20);
    const int b0 = ((tbp * 2 + tb) * 32 + 8 * w) * 1024;
    {
      char* img = lds + w * 16384;
#pragma unroll
      for (int i = 0; i < 16; i++) *(u32x4*)(img + i * 1024 + lo) = wraw[i];
      const int h = lane >> 5, blk = (lane >> 4) & 1, q = (lane & 15) >> 2, p = lane & 3;
      const char* pp = img + (8 * h + q) * 128 + blk * 32 + p * 8;
      const int j0 = (kh ^ (q >> 1)) * 64;
#pragma unroll
      for (int s = 0; s < 8; s++) {
        r[s][0] = tr_frag(pp + s * 2048 + j0, pp + s * 2048 + 512 + j0);
        r[s][1] = tr_frag(pp + s * 2048 + (j0 ^ 64), pp + s * 2048 + 512 + (j0 ^ 64));
      }
    }
    if (w == 0) await(ws, FL_LN + rep * 1024 + lo, ep);
    __syncthreads();
#pragma unroll
    for (int s = 0; s < 8; s++) { r[s][2] = bld<16>(rb, lo, b0 + s * 1024); r[s][3] = bld<16>(rb, lo, (b0 ^ 32768) + s * 1024); }
    const u32x4 k0 = LDK(0), k1 = LDK(1);
    __builtin_amdgcn_sched_barrier(0);
    fetch(0, 0);
    __builtin_amdgcn_sched_barrier(0);
#pragma unroll
    for (int s = 0; s < 8; s++) {
      acc[0][0] = MFMA32(r[s][0], r[s][2], acc[0][0]);
      acc[1][0] = MFMA32(r[s][1], r[s][2], acc[1][0]);
      acc[0][1] = MFMA32(r[s][0], r[s][3], acc[0][1]);
      acc[1][1] = MFMA32(r[s][1], r[s][3], acc[1][1]);
      asm volatile("" : "+v"(acc[0][0]), "+v"(acc[1][0]), "+v"(acc[0][1]), "+v"(acc[1][1]));
      __builtin_amdgcn_sched_barrier(0);
    }
#pragma unroll
    for (int x = 1; x < 4; x++) {
      const f32x16& a = acc[x & 1][x >> 1];
      char* p = lds + SLOT + (((w ^ x) * 3 + x - 1) * 4) * 1024 + lo;
#pragma unroll
      for (int q = 0; q < 4; q++) *(f32x4*)(p + q * 1024) = (f32x4){a[4 * q], a[4 * q + 1], a[4 * q + 2], a[4 * q + 3]};
    }
    o0 = zero16w(); o1 = zero16w(); o2 = zero16w(); o3 = zero16w(); s3 = zero16w();
    __syncthreads();
    const float sc = 0.25f * 1.4426950408889634f;
    unsigned qq[8];
    f32x4 pr[12];
#pragma unroll
    for (int i = 0; i < 12; i++) pr[i] = *(const f32x4*)(lds + SLOT + (w * 12 + (i % 3) * 4 + i / 3) * 1024 + lo);
    __builtin_amdgcn_sched_barrier(0);
#pragma unroll
    for (int q = 0; q < 4; q++) {
      f32x4 a = pr[3 * q] + pr[3 * q + 1] + pr[3 * q + 2];
#pragma unroll
      for (int e = 0; e < 4; e++) a[e] = (a[e] + acc[0][0][4 * q + e]) * sc;
      qq[2 * q] = pk(a[0], a[1]); qq[2 * q + 1] = pk(a[2], a[3]);
    }
    q0 = (u32x4){qq[0], qq[1], qq[2], qq[3]}; q1 = (u32x4){qq[4], qq[5], qq[6], qq[7]};
    PIN(q0); PIN(q1);
    const f32x16 z = splat16(0.f);
    const f32x16 t0 = MFMA32(k0, q0, z), t1 = MFMA32(k1, q0, z), t2 = MFMA32(k0, q1, z), t3 = MFMA32(k1, q1, z);
    m0 = fmx(max16(t0), max16(t1));
    m1 = fmx(max16(t2), max16(t3));
    m0 = max_halves(m0) + 64.f;
    m1 = max_halves(m1) + 64.f;
    char* xq = lds + 4 * SLOT;
    *(u32x4*)(xq + (w * 3) * 1024 + lo) = q0;
    *(u32x4*)(xq + (w * 3 + 1) * 1024 + lo) = q1;
    *(f32x2*)(xq + (w * 3 + 2) * 1024 + lane * 8) = (f32x2){m0, m1};
    const u32x4 fill = (t & 1) ? (u32x4){0, 0, 0, 0} : ones;
    if (t < 16) *(u32x4*)(lds + (t >> 3) * HALF + 4096 + ((t >> 1) & 3) * VP + 1024 + (t & 1) * 16) = fill;
    c0 = splat16w(-m0); c1 = splat16w(-m1);
    asm volatile("s_waitcnt vmcnt(0)" : "+v"(c0), "+v"(c1) :: "memory");
    __syncthreads();
    kf = *(const u32x4*)kp; va = *(const u32x4*)vp0; vb = va;
    q2 = *(const u32x4*)(xq + ((w ^ 1) * 3) * 1024 + lo);
    q3 = *(const u32x4*)(xq + ((w ^ 1) * 3 + 1) * 1024 + lo);
    const f32x2 mm = *(const f32x2*)(xq + ((w ^ 1) * 3 + 2) * 1024 + lane * 8);
    m2 = mm[0]; m3 = mm[1];
    __builtin_amdgcn_sched_barrier(0);
    fetch(1, 1); fetch(2, 2);
    if (t < 48) *(u32x4*)(lds + SLOT + (t >> 3) * HALF + 4096 + ((t >> 1) & 3) * VP + 1024 + (t & 1) * 16) = fill;
  }
  {
    const f32x16 c2 = splat16w(-m2), c3 = splat16w(-m3);
    f32x16 s0 = MFMA32(kf, q0, c0), s1 = MFMA32(kf, q1, c1), s2 = MFMA32(kf, q2, c2);
#define EXPN(s, o, n) do { _Pragma("unroll") for (int i_ = 0; i_ < n; i_++) { float e_ = __builtin_amdgcn_exp2f(s[(o) + i_]); PIN(e_); s[(o) + i_] = e_; } } while (0)
#define LO8(s) __builtin_shufflevector(s, s, 0, 1, 2, 3, 4, 5, 6, 7)
#define HI8(s) __builtin_shufflevector(s, s, 8, 9, 10, 11, 12, 13, 14, 15)
#define TILE(X, OX, Y, OY, QY, CY, H0, H1, H2) do { \
      EXPN(X, 0, 3); OY = SMF(vb, HI8(Y), OY); PIN(OY); H0; \
      EXPN(X, 3, 5); Y = MFMA32(kf, QY, CY); PIN(Y); H1; \
      EXPN(X, 8, 5); OX = SMF(va, LO8(X), OX); PIN(OX); H2; \
      EXPN(X, 13, 3); } while (0)
    auto chunk = [&](int c, auto first) {
      const int cur = (c & 3) * SLOT, nxt = ((c + 1) & 3) * SLOT;
#pragma unroll
      for (int i = 0; i < 4; i++) {
        if (i == 3) vm_barrier<first ? 4 : 0>();
        const int o1_ = i < 3 ? cur : nxt, i1 = (i + 1) & 3;
        TILE(s0, o0, s3, o3, q3, c3, vb = *(const u32x4*)(vp1 + cur + i * VP), kf = *(const u32x4*)(kp + o1_ + i1 * 1024),
             if (first && i == 3) fetch1(c + 3, (c + 3) & 3, 0));
        TILE(s1, o1, s0, o0, q0, c0, , , if (first && i == 3) fetch1(c + 3, (c + 3) & 3, 1));
        TILE(s2, o2, s1, o1, q1, c1, , , if (first && i == 3) fetch1(c + 3, (c + 3) & 3, 2));
        TILE(s3, o3, s2, o2, q2, c2, if (first && i == 3) fetch1(c + 3, (c + 3) & 3, 3), , va = *(const u32x4*)(vp0 + o1_ + i1 * VP));
      }
    };
    for (int c = 0; c < 5; c++) chunk(c, std::true_type{});
    for (int c = 5; c < 8; c++) chunk(c, std::false_type{});
    o3 = SMF(vb, HI8(s3), o3);
  }
  const rsrc_t ors = mkrsrc(ws + OFF_O, 2 << 20);
  bool second = false;
  for (;;) {
#pragma unroll
    for (int e = 0; e < 2; e++) {
      const f32x16 s = e ? o3 : o2;
      char* p = lds + ((w * 2 + e) * 3) * 1024;
      *(f32x4*)(p + lo) = (f32x4){s[0], s[1], s[2], s[3]};
      *(f32x4*)(p + 1024 + lo) = (f32x4){s[4], s[5], s[6], s[7]};
      *(float*)(p + 2048 + lane * 4) = s[8];
    }
    __syncthreads();
    bool bad = false;
    f32x4 pa[2], pb[2];
    float pl[2];
#pragma unroll
    for (int e = 0; e < 2; e++) {
      const char* p = lds + (((w ^ 1) * 2 + e) * 3) * 1024;
      pa[e] = *(const f32x4*)(p + lo); pb[e] = *(const f32x4*)(p + 1024 + lo); pl[e] = *(const float*)(p + 2048 + lane * 4);
    }
    __builtin_amdgcn_sched_barrier(0);
#pragma unroll
    for (int e = 0; e < 2; e++) {
      const f32x16 s = e ? o1 : o0;
      const float l = s[8] + pl[e], il = __builtin_amdgcn_rcpf(l);
      float r[8], am = 0.f;
#pragma unroll
      for (int i = 0; i < 4; i++) {
        r[i] = s[i] + pa[e][i]; r[4 + i] = s[4 + i] + pb[e][i];
        am += fabsf(r[i]) + fabsf(r[4 + i]);
      }
      st128(ors, lo + ((tbp * 2 + tb) * 32 + hg * 4 + 2 * kh + e) * 1024,
            (u32x4){pk(r[0] * il, r[1] * il), pk(r[2] * il, r[3] * il), pk(r[4] * il, r[5] * il), pk(r[6] * il, r[7] * il)});
      bad |= !(l >= 0x1p-100f && l <= 0x1p120f && am <= 0x1p126f);
    }
    int* vote = (int*)(lds + 30720);
    const bool anybad = __builtin_amdgcn_ballot_w64(bad) != 0;
    if (lane == 0) vote[w] = !second && anybad;
    asm volatile("s_waitcnt vmcnt(0)" ::: "memory");
    __syncthreads();
    const u32x4 vt = *(const u32x4*)vote;
    if (__builtin_amdgcn_readfirstlane(vt.x | vt.y | vt.z | vt.w) == 0) break;
    second = true;
    float* mx = (float*)(lds + 32768);
    for (int hd = 0; hd < 4; hd++) {
      const u32x4 q = hd == 0 ? q0 : hd == 1 ? q1 : hd == 2 ? q2 : q3;
      const f32x16 z = splat16(0.f);
      float m = -3.0e38f;
      for (int kb = kb0; kb < kb0 + 32; kb++) m = fmaxf(m, max16(MFMA32(LDK(kb), q, z)));
      mx[(hd * 4 + w) * 64 + lane] = max_halves(m);
    }
    __syncthreads();
    for (int hd = 0; hd < 4; hd++) {
      const u32x4 q = hd == 0 ? q0 : hd == 1 ? q1 : hd == 2 ? q2 : q3;
      const float m = fmaxf(mx[(hd * 4 + w) * 64 + lane], mx[((hd ^ 2) * 4 + (w ^ 1)) * 64 + lane]);
      const f32x16 c = splat16(-m);
      f32x16 oo = splat16(0.f);
      for (int kb = kb0; kb < kb0 + 32; kb++) {
        const f32x16 s = MFMA32(LDK(kb), q, c);
        f32x8 p0, p1;
        const int vo = (lane >> 5) * 256 + (m_ & 15) * 16;
        const u32x4 v0 = bld<16>(vrs, vo, kb * 1024), v1 = bld<16>(vrs, vo, kb * 1024 + 512);
        exp8(p0, s, 0); oo = SMF(m_ < 16 ? v0 : ones, p0, oo);
        exp8(p1, s, 8); oo = SMF(m_ < 16 ? v1 : ones, p1, oo);
      }
      if (hd == 0) o0 = oo; else if (hd == 1) o1 = oo; else if (hd == 2) o2 = oo; else o3 = oo;
    }
  }
  if (t == 0) st64(mkrsrc(ws + OFF_FLAG, FL_BYTES), FL_O + (tbp * 4 + (hg >> 1)) * 128 + (hg & 1) * 8, ep);
#undef LDK
}

constexpr int UPOFF = 32 * 224 + 128, STAGE = 2 * 32 * 224 + 128;
DEV void gemm1(char* lds, const u16* __restrict__ wg, const u16* __restrict__ wu, char* __restrict__ ws, int mt, int nt, int pfx, u32x2& ep) {
  const int t = threadIdx.x, lane = t & 63, w = __builtin_amdgcn_readfirstlane(t >> 6);
  const u32x4 wsrd = mksrd((w & 2) ? wu : wg, 512 * 3584), xsrd = mksrd(ws + OFF_XN, 2 << 20);
  int go[4];
#pragma unroll
  for (int i = 0; i < 4; i++) { const int id = i * 64 + lane, row = id / 14; go[i] = row * 3584 + (id - row * 14) * 16; }
  const int wsrc = nt * 224 + (w & 1) * 16 * 3584, lo = lane * 16, tb = mt * 4 + w;
  const unsigned ldst = lds_addr(lds) + (w >> 1) * UPOFF + (w & 1) * 3584;
  const int h = lane >> 5, blk = (lane >> 4) & 1, q = (lane & 15) >> 2, p = lane & 3;
  const char* pa = lds + blk * UPOFF + (8 * h + q) * 224 + p * 8;
  f32x16 acc[7];
#pragma unroll
  for (int i = 0; i < 7; i++) acc[i] = splat16(0.f);
  u32x4 bb[5][2], f[7];
  auto issue1 = [&](int c, int i) {
    const int st = c % 5;
    if (i < 3) dma(ldst + st * STAGE + i * 1024, go[i], wsrd, wsrc + c * (32 * 3584));
    else if (i == 3) dma_half(ldst + st * STAGE + 3072, go[3], wsrd, wsrc + c * (32 * 3584));
    else if (i < 6)
      asm volatile("buffer_load_dwordx4 %0, %1, %2, %3 offen sc1" : "=v"(bb[st][i - 4]) : "v"(lo), "s"(xsrd), "s"(tb * 32768 + (c * 2 + i - 4) * 1024) : "memory");
  };
  auto issue = [&](int c) {
#pragma unroll
    for (int i = 0; i < 6; i++) issue1(c, i);
  };
  auto rd = [&](int c, int s, int j) {
    const char* pp = pa + (c % 5) * STAGE + s * 3584 + j * 32;
    return tr_frag(pp, pp + 896);
  };
#pragma unroll
  for (int c = 0; c < 4; c++)
#pragma unroll
    for (int i = 0; i < 4; i++) issue1(c, i);
  ep = next_epoch(ws);
  await(ws, FL_LN + pfx * 1024 + tb * 16, ep);
#pragma unroll
  for (int c = 0; c < 4; c++) { issue1(c, 4); issue1(c, 5); }
  if (GHOLD) __builtin_amdgcn_s_sleep(GHOLD);
  const u32x4 fsrd = mksrd(ws + OFF_FLAG, FL_BYTES);
  const int rto = FL_RT + min(lane, 35) * 16;
  u32x4 rtf;
  vm_barrier<6>();
  asm volatile("" : "+v"(bb[0][0]), "+v"(bb[0][1]));
#pragma unroll
  for (int j = 0; j < 7; j++) f[j] = rd(0, 0, j);
#pragma unroll
  for (int c = 0; c < 16; c++) {
#pragma unroll
    for (int j = 0; j < 7; j++) {
      acc[j] = MFMA32(f[j], bb[c % 5][0], acc[j]); PIN(acc[j]);
      f[j] = rd(c, 1, j);
    }
    if (c < 15) {
      if (c == 0) vm_barrier<4>(); else if (c == 1) vm_barrier<8>(); else if (c < 13) vm_barrier<12>(); else if (c == 13) vm_barrier<6>(); else vm_barrier<0>();
      asm volatile("" : "+v"(bb[(c + 1) % 5][0]), "+v"(bb[(c + 1) % 5][1]));
      if (!SPREAD && c < 12) issue(c + 4);
      __builtin_amdgcn_sched_barrier(0);
    }
#pragma unroll
    for (int j = 0; j < 7; j++) {
      acc[j] = MFMA32(f[j], bb[c % 5][1], acc[j]); PIN(acc[j]);
      if (c < 15) f[j] = rd(c + 1, 0, j);
      if (SPREAD && c < 12) { issue1(c + 4, j); __builtin_amdgcn_sched_barrier(0); }
    }
    if (c == 12) asm volatile("buffer_load_dwordx4 %0, %1, %2, 0 offen sc1" : "=v"(rtf) : "v"(rto), "s"(fsrd) : "memory");
    if ((c & 3) == 3) { }
  }
  __builtin_amdgcn_s_setprio(P_PUT);
  const rsrc_t hrs = mkrsrc(ws + OFF_H, 7168 << 10);
  auto put = [&](const f32x16& a, int tb, int fb) {
    float hv[8];
#pragma unroll
    for (int r = 0; r < 8; r++) { const float gt = a[r]; hv[r] = BC(float, max(BC(int, gt), 0)) * a[8 + r]; }
    __builtin_amdgcn_raw_buffer_store_b128((u32x4){pk(hv[0], hv[1]), pk(hv[2], hv[3]), pk(hv[4], hv[5]), pk(hv[6], hv[7])}, hrs,
                                           lane * 16 + (tb * 112 + fb) * 1024, 0, 16);
  };
#pragma unroll
  for (int j = 0; j < 7; j++) put(acc[j], tb, nt * 7 + j);
  asm volatile("" : "+v"(rtf));
  if (!raised(rtf, ep)) await(ws, rto, ep);
}

DEV void tail(char* lds, const u16* __restrict__ x, char* __restrict__ ws, u16* __restrict__ out, int hidx, int tbp, int ct, u32x2 ep) {
  const int t = threadIdx.x, lane = t & 63, w = __builtin_amdgcn_readfirstlane(t >> 6), lo = lane * 16;
  const int cg = t & 7, tk = t >> 3;
  f32x16 acc[2][2];
  u32x4 r[8][4];
  float g[2][8];
  {
    const rsrc_t ra = mkrsrc(ws + OFF_WD, 1792 << 10), rb = mkrsrc(ws + OFF_H, 7168 << 10);
    const int a0 = (ct * 2 * 112 + 28 * w) * 1024, b0 = (tbp * 2 * 112 + 28 * w) * 1024;
    signal(ws, FL_H + (hidx >> 2) * 128 + (hidx & 3) * 8, ep);
    __builtin_amdgcn_s_setprio(P_MID);
    tileA<8>(r, ra, a0, a0 + 112 * 1024, lo);
    zero4(acc);
    await(ws, FL_H + ((tbp >> 1) * 4 + w) * 128 + (lane & 1) * 16, ep);
    __builtin_amdgcn_s_setprio(GPRIO);
    tile64<28, 8, 16>(r, ra, a0, a0 + 112 * 1024, rb, b0, b0 + 112 * 1024, lo, acc);
    __builtin_amdgcn_s_setprio(P_SILU);
  }
  const rsrc_t ra = mkrsrc(ws + OFF_WO, 512 << 10), rb = mkrsrc(ws + OFF_O, 2 << 20);
  const int a0 = (ct * 2 * 32 + 8 * w) * 1024, b0 = (tbp * 2 * 32 + 8 * w) * 1024;
  tileA<8>(r, ra, a0, a0 + 32768, lo);
  u32x4 xv[2];
#pragma unroll
  for (int j = 0; j < 2; j++) xv[j] = *(const u32x4*)(x + (tbp * 64 + j * 32 + tk) * 512 + ct * 64 + cg * 8);
  red_write(lds, w, lane, acc);
  __syncthreads();
#pragma unroll
  for (int j = 0; j < 2; j++) {
    red_read8(lds, j, cg, tk, g[j]);
#pragma unroll
    for (int e = 0; e < 8; e++) {
      g[j][e] = g[j][e] * __builtin_amdgcn_rcpf(1.0f + __expf(-g[j][e]));
      asm volatile("" : "+v"(g[j][e]));
    }
  }
  zero4(acc);
  __syncthreads();
  await(ws, FL_O + (tbp * 4 + w) * 128, ep);
  __builtin_amdgcn_s_setprio(P_FIN);
  tile64<8, 8, 16>(r, ra, a0, a0 + 32768, rb, b0, b0 + 32768, lo, acc);
  red_write(lds, w, lane, acc);
  __syncthreads();
  f32x4 pr[2][4][2];
#pragma unroll
  for (int j = 0; j < 2; j++)
#pragma unroll
    for (int u = 0; u < 4; u++) {
      const char* p = lds + ((u * 2 + j) * 8 + cg) * RP + tk * 16;
      pr[j][u][0] = *(const f32x4*)p; pr[j][u][1] = *(const f32x4*)(p + 512);
    }
  __builtin_amdgcn_sched_barrier(0);
#pragma unroll
  for (int j = 0; j < 2; j++) {
    float s[8], o[8];
    const f32x4 sa = (pr[j][0][0] + pr[j][1][0]) + (pr[j][2][0] + pr[j][3][0]), sb = (pr[j][0][1] + pr[j][1][1]) + (pr[j][2][1] + pr[j][3][1]);
#pragma unroll
    for (int e = 0; e < 4; e++) { s[e] = sa[e]; s[4 + e] = sb[e]; }
#pragma unroll
    for (int e = 0; e < 8; e++) o[e] = ((e & 1) ? hi16(xv[j][e >> 1]) : lo16(xv[j][e >> 1])) + s[e] * g[j][e];
    __builtin_amdgcn_raw_buffer_store_b128((u32x4){pk(o[0], o[1]), pk(o[2], o[3]), pk(o[4], o[5]), pk(o[6], o[7])},
                                           mkrsrc((const char*)out, 2 << 20), ((tbp * 64 + j * 32 + tk) * 512 + ct * 64 + cg * 8) * 2, 0, OAUX);
  }
}

__global__ __launch_bounds__(256, 2) void k2(const u16* __restrict__ x, const u16* __restrict__ wq, const u16* __restrict__ wk,
                                             const u16* __restrict__ wv, const u16* __restrict__ wo, const u16* __restrict__ wg,
                                             const u16* __restrict__ wu, const u16* __restrict__ wd, char* __restrict__ ws,
                                             u16* __restrict__ out) {
  __shared__ __attribute__((aligned(16))) char lds[LDS2];
  const int b = blockIdx.x, xc = b & 7, r = (b >> 3) & 31, lane = threadIdx.x & 63;
  if (__builtin_expect(b < 256, 1)) {
    __builtin_amdgcn_s_setprio(APRIO);
    const u32x2 ep0 = BC(u32x2, __builtin_amdgcn_raw_buffer_load_b64(mkrsrc(ws + OFF_FLAG, FL_BYTES), FL_EPOCH, 0, 16));
    const int tbp = (xc & 1) * 16 + (r & 15), hg = (xc >> 1) * 2 + (r >> 4);
    u32x4 wraw[16];
    const rsrc_t qrs = mkrsrc((const char*)wq, 512 * 1024);
    const int qo = ((threadIdx.x >> 6) * 128 + (lane >> 3)) * 1024 + hg * 128 + (((lane & 7) ^ (((lane >> 4) & 1) * 4)) * 16);
    auto ldq = [&]() {
#pragma unroll
      for (int i = 0; i < 16; i++) wraw[i] = bld<0>(qrs, qo, i * 8192);
    };
    if (b >= 128 && DLY) __builtin_amdgcn_s_sleep(DLY);
    if (__builtin_expect(b < 128, 1)) { if (LATEQ) layernorm(lds, x, wk, wv, ws, b, []() {}); else layernorm(lds, x, wk, wv, ws, b, ldq); }
    else if (b < 200) retile<2>(lds, wo, wd, ws, 2 * b - 256, ldq);
    else ldq();
    const unsigned e0 = ep0.x, e1 = ep0.y;
    const uint64_t e = (((uint64_t)(unsigned)__builtin_amdgcn_readfirstlane(e1) << 32) | (unsigned)__builtin_amdgcn_readfirstlane(e0)) + 1;
    const u32x2 ep = {(unsigned)e, (unsigned)(e >> 32)};
    if (b < 128) { signal(ws, FL_LN + b * 8, ep, 8); if (LATEQ) ldq(); }
    else if (b < 200) signal(ws, FL_RT + (b - 128) * 8, ep);
    attention(lds, ws, tbp, hg, xc, ep, wraw);
  } else {
    const int mt = (xc & 1) * 8 + (r & 7), nt = (xc >> 1) * 4 + (r >> 3);
    __builtin_amdgcn_s_setprio(GPRIO);
    if (DLYG) __builtin_amdgcn_s_sleep(DLYG);
    u32x2 ep;
    gemm1(lds, wg, wu, ws, mt, nt, xc, ep);
    tail(lds, x, ws, out, mt * 16 + nt, xc * 4 + (r & 3), r >> 2, ep);
    if (b == 511 && threadIdx.x < 64) {
#pragma unroll 1
      for (int i = 0; i < 2; i++) {
        await(ws, FL_H + lane * 128 + i * 16, ep);
        await(ws, FL_O + (i * 64 + lane) * 128, ep);
      }
      if (lane == 0) st64(mkrsrc(ws + OFF_FLAG, FL_BYTES), FL_EPOCH, ep);
    }
  }
}

extern "C" void kernel_launch(void* const* d_in, const int* in_sizes, int n_in, void* d_out, int out_size, void* d_ws,
                              size_t ws_size, hipStream_t stream) {
  k2<<<512, 256, 0, stream>>>((const u16*)d_in[0], (const u16*)d_in[1], (const u16*)d_in[2], (const u16*)d_in[3],
                              (const u16*)d_in[4], (const u16*)d_in[6], (const u16*)d_in[7], (const u16*)d_in[8], (char*)d_ws,
                              (u16*)d_out);
}
```
